```python
import torch

def _softmax(x, axis=-1):
    return torch.softmax(x, dim=axis)

def _gelu(x):
    return torch.nn.functional.gelu(x, approximate='tanh')

def _silu(x):
    return torch.nn.functional.silu(x)

def _sigmoid(x):
    return torch.sigmoid(x)

def _layernorm(x, axis=-1, eps=1e-05):
    m = x.mean(dim=axis, keepdim=True)
    v = x.var(dim=axis, keepdim=True, unbiased=False)
    return (x - m) / torch.sqrt(v + eps)

def _scatter_add(tgt, idx, src, axis=0):
    return tgt.clone().index_add_(axis, idx, src)

def _segment_sum(src, seg, n):
    out = torch.zeros((n,) + src.shape[1:], dtype=src.dtype)
    return out.index_add_(0, seg, src)

def _pad(x, pw):
    flat = tuple((v for pair in reversed(pw) for v in pair))
    return torch.nn.functional.pad(x, flat)
kernel_name = 'adaLN_cond__spectral_fnet__moe_arch_mod__prompt'

def setup_inputs():

    _w = lambda a: (lambda t: t.to(torch.bfloat16) if t.is_floating_point() else t)(torch.as_tensor(a))

    def _A_setup():
        B, S, D = (1, 2048, 448)
        ns = torch.arange(S)[:, None] * torch.arange(S)[None, :]
        nd = torch.arange(D)[:, None] * torch.arange(D)[None, :]
        Ws_re = torch.cos(-2.0 * torch.pi * ns / S).to(float)
        Ws_im = torch.sin(-2.0 * torch.pi * ns / S).to(float)
        Wd_re = torch.cos(-2.0 * torch.pi * nd / D).to(float)
        Wd_im = torch.sin(-2.0 * torch.pi * nd / D).to(float)
        return {'x': _w((lambda *s: torch.randn(*s, dtype=torch.bfloat16) * 0.05)(B, S, D)), 'dft_seq_re': _w(Ws_re), 'dft_seq_im': _w(Ws_im), 'dft_hid_re': _w(Wd_re), 'dft_hid_im': _w(Wd_im)}
    _A_in = _A_setup()

    def _B_setup():
        B, S, D, F, C = (1, 2048, 448, 1792, 1)
        return {'x': _w((lambda *s: torch.randn(*s, dtype=torch.bfloat16) * 0.05)(B, S, D)), 'w_r': _w((lambda *s: torch.randn(*s, dtype=torch.bfloat16) * 3.2)(D)), 'w_gate': _w((lambda *s: torch.randn(*s, dtype=torch.bfloat16) * 0.8)(D, F)), 'w_up': _w((lambda *s: torch.randn(*s, dtype=torch.bfloat16) * 0.8)(D, F)), 'w_down': _w((lambda *s: torch.randn(*s, dtype=torch.bfloat16) * 0.8)(F, D))}
    _B_in = _B_setup()
    return {'A_x': _A_in['x'], 'A_dft_seq_re': _A_in['dft_seq_re'], 'A_dft_seq_im': _A_in['dft_seq_im'], 'A_dft_hid_re': _A_in['dft_hid_re'], 'A_dft_hid_im': _A_in['dft_hid_im'], 'B_x': _B_in['x'], 'B_w_r': _B_in['w_r'], 'B_w_gate': _B_in['w_gate'], 'B_w_up': _B_in['w_up'], 'B_w_down': _B_in['w_down'], 'c': _w((lambda *s: torch.randn(*s, dtype=torch.bfloat16) * 0.1)(1, 448) * 8.0), 'w_mod': _w((lambda *s: torch.randn(*s, dtype=torch.bfloat16) * 0.1)(448, 6 * 448) * 448 ** (-0.5) * 8.0)}

def reference(A_x, A_dft_seq_re, A_dft_seq_im, A_dft_hid_re, A_dft_hid_im, B_x, B_w_r, B_w_gate, B_w_up, B_w_down, c, w_mod):

    def _A_ref(x, dft_seq_re, dft_seq_im, dft_hid_re, dft_hid_im):
        a = torch.einsum('bsd,ed->bse', x, dft_hid_re)
        b = torch.einsum('bsd,ed->bse', x, dft_hid_im)
        y = torch.einsum('ts,bse->bte', dft_seq_re, a) - torch.einsum('ts,bse->bte', dft_seq_im, b)
        return y

    def _B_ref(x, w_r, w_gate, w_up, w_down):
        B, S, D = x.shape
        C = 1
        r = x @ w_r
        idx = torch.argsort(-r, axis=-1)[:, :C]
        idx_e = (lambda x, n, axis: torch.repeat_interleave(x, n, dim=axis))(idx.reshape(B, C, 1), D, axis=2)
        x_sel = (lambda x, i, axis: torch.take_along_dim(x, i, dim=axis))(x, idx_e, axis=1)
        g = _silu(x_sel @ w_gate)
        u = x_sel @ w_up
        y = g * u @ w_down
        r_sel = _sigmoid((lambda x, i, axis: torch.take_along_dim(x, i, dim=axis))(r, idx, axis=1))
        y = y * r_sel.reshape(B, C, 1)
        b_off = (torch.arange(B) * S).reshape(B, 1)
        flat_idx = (idx + b_off).reshape(B * C)
        zero = (x * 0.0).reshape(B * S, D)
        upd = _scatter_add(zero, flat_idx, y.reshape(B * C, D), axis=0)
        return x + upd.reshape(B, S, D)
    x = A_x
    mod = (_silu(c) @ w_mod).reshape(c.shape[0], 6, 1, -1)
    sa, ca, ga, sb, cb, gb = (mod[:, 0].clone(), mod[:, 1].clone(), mod[:, 2].clone(), mod[:, 3].clone(), mod[:, 4].clone(), mod[:, 5].clone())
    h = _layernorm(x, axis=-1) * (1 + ca) + sa
    x = x + ga * _A_ref(h, A_dft_seq_re, A_dft_seq_im, A_dft_hid_re, A_dft_hid_im)
    h = _layernorm(x, axis=-1) * (1 + cb) + sb
    x = x + gb * _B_ref(h, B_w_r, B_w_gate, B_w_up, B_w_down)
    return x
```

```cpp
#include <hip/hip_runtime.h>
#include <hip/hip_bf16.h>
#include <stdint.h>

typedef unsigned short u16;
typedef __attribute__((ext_vector_type(8))) short bf16x8;
typedef __attribute__((ext_vector_type(4))) float f32x4;

static constexpr int S = 2048, D = 448, F = 1792;

__device__ __forceinline__ float bf2f(u16 v) { return __uint_as_float(((unsigned)v) << 16); }
__device__ __forceinline__ u16 f2bf(float f) { __hip_bfloat16 h = __float2bfloat16(f); return *(u16*)&h; }
__device__ __forceinline__ float rbf(float f) { return bf2f(f2bf(f)); }

__global__ void k_mod(const u16* c, const u16* wmod, float* mod) {
  int j = blockIdx.x * 64 + threadIdx.x;
  float acc = 0;
  for (int k = 0; k < D; ++k) {
    float cv = bf2f(c[k]);
    float s = rbf(cv / (1.f + __expf(-cv)));
    acc += s * bf2f(wmod[k * 6 * D + j]);
  }
  mod[j] = rbf(acc);
}

__device__ __forceinline__ float wsum(float v) {
  for (int o = 32; o; o >>= 1) v += __shfl_xor(v, o);
  return v;
}

__global__ void k_ln1(const u16* x, const float* mod, u16* h) {
  int row = blockIdx.x * 4 + (threadIdx.x >> 6), lane = threadIdx.x & 63;
  float v[7]; float s = 0;
  for (int i = 0; i < 7; ++i) { v[i] = bf2f(x[row * D + i * 64 + lane]); s += v[i]; }
  float m = wsum(s) / D; float q = 0;
  for (int i = 0; i < 7; ++i) { float d = v[i] - m; q += d * d; }
  float rs = rsqrtf(wsum(q) / D + 1e-5f);
  for (int i = 0; i < 7; ++i) { int e = i * 64 + lane;
    h[row * D + e] = f2bf((v[i] - m) * rs * (1.f + mod[D + e]) + mod[e]); }
}

__global__ void k_gemm1(const u16* h, const u16* wre, const u16* wim, u16* abT) {
  int lane = threadIdx.x & 63;
  int tm = blockIdx.x, tn = blockIdx.y * 4 + (threadIdx.x >> 6);
  const u16* W = tn < 28 ? wre : wim; int n0 = (tn % 28) * 16;
  f32x4 acc = {0, 0, 0, 0};
  for (int k = 0; k < D; k += 32) {
    bf16x8 a = *(const bf16x8*)(h + (tm * 16 + (lane & 15)) * D + k + (lane >> 4) * 8);
    bf16x8 b = *(const bf16x8*)(W + (n0 + (lane & 15)) * D + k + (lane >> 4) * 8);
    acc = __builtin_amdgcn_mfma_f32_16x16x32_bf16(__builtin_bit_cast(__attribute__((ext_vector_type(8))) __bf16, a), __builtin_bit_cast(__attribute__((ext_vector_type(8))) __bf16, b), acc, 0, 0, 0);
  }
  int e = n0 + (lane & 15);
  for (int j = 0; j < 4; ++j) { int s = tm * 16 + (lane >> 4) * 4 + j;
    float v = acc[j]; if (tn >= 28) v = -v;
    abT[e * 2 * S + (tn < 28 ? 0 : S) + s] = f2bf(v); }
}

__global__ void k_gemm2(const u16* sre, const u16* sim, const u16* abT, float* y) {
  int lane = threadIdx.x & 63;
  int tm = blockIdx.x, tn = blockIdx.y * 4 + (threadIdx.x >> 6);
  f32x4 acc = {0, 0, 0, 0};
  for (int k = 0; k < 2 * S; k += 32) {
    const u16* A = k < S ? sre : sim; int kk = k & (S - 1);
    bf16x8 a = *(const bf16x8*)(A + (tm * 16 + (lane & 15)) * S + kk + (lane >> 4) * 8);
    bf16x8 b = *(const bf16x8*)(abT + (tn * 16 + (lane & 15)) * 2 * S + k + (lane >> 4) * 8);
    acc = __builtin_amdgcn_mfma_f32_16x16x32_bf16(__builtin_bit_cast(__attribute__((ext_vector_type(8))) __bf16, a), __builtin_bit_cast(__attribute__((ext_vector_type(8))) __bf16, b), acc, 0, 0, 0);
  }
  for (int j = 0; j < 4; ++j) y[(tm * 16 + (lane >> 4) * 4 + j) * D + tn * 16 + (lane & 15)] = acc[j];
}

__global__ void k_ep(const u16* x, const float* y, const float* mod, const u16* wr, u16* out, float* x2o, float* h2o, float* r) {
  int row = blockIdx.x * 4 + (threadIdx.x >> 6), lane = threadIdx.x & 63;
  float v[7]; float s = 0;
  for (int i = 0; i < 7; ++i) { int e = i * 64 + lane; v[i] = bf2f(x[row * D + e]) + mod[2 * D + e] * y[row * D + e]; s += v[i]; }
  float m = wsum(s) / D; float q = 0;
  for (int i = 0; i < 7; ++i) { float d = v[i] - m; q += d * d; }
  float rs = rsqrtf(wsum(q) / D + 1e-5f); float rr = 0;
  for (int i = 0; i < 7; ++i) { int e = i * 64 + lane;
    float h2 = (v[i] - m) * rs * (1.f + mod[4 * D + e]) + mod[3 * D + e];
    rr += h2 * bf2f(wr[e]); x2o[row * D + e] = v[i]; h2o[row * D + e] = h2;
    out[row * D + e] = f2bf(v[i] + mod[5 * D + e] * h2); }
  rr = wsum(rr);
  if (lane == 0) r[row] = rr;
}

__global__ void k_ffn1(const float* r, const float* h2, const u16* wg, const u16* wu, float* gu, int* idxo) {
  __shared__ float sv[256]; __shared__ int si[256]; __shared__ float xs[D];
  int t = threadIdx.x; float bv = -INFINITY; int bi = 0;
  for (int i = t; i < S; i += 256) { float v = r[i]; if (v > bv) { bv = v; bi = i; } }
  sv[t] = bv; si[t] = bi; __syncthreads();
  for (int o = 128; o; o >>= 1) { if (t < o) { if (sv[t + o] > sv[t] || (sv[t + o] == sv[t] && si[t + o] < si[t])) { sv[t] = sv[t + o]; si[t] = si[t + o]; } } __syncthreads(); }
  int idx = si[0];
  for (int i = t; i < D; i += 256) xs[i] = rbf(h2[idx * D + i]);
  __syncthreads();
  int j = blockIdx.x * 256 + t;
  float g = 0, u = 0;
  for (int k = 0; k < D; ++k) { g += xs[k] * bf2f(wg[k * F + j]); u += xs[k] * bf2f(wu[k * F + j]); }
  g = rbf(g); u = rbf(u);
  gu[j] = rbf(rbf(g / (1.f + __expf(-g))) * u);
  if (blockIdx.x == 0 && t == 0) *idxo = idx;
}

__global__ void k_ffn2(const float* gu, const u16* wd, const float* r, const int* idxp, const float* x2, const float* h2, const float* mod, u16* out) {
  int e = blockIdx.x * 64 + threadIdx.x; int idx = *idxp;
  float acc = 0;
  for (int k = 0; k < F; ++k) acc += gu[k] * bf2f(wd[k * D + e]);
  float rv = r[idx]; float sg = 1.f / (1.f + __expf(-rv));
  out[idx * D + e] = f2bf(x2[idx * D + e] + mod[5 * D + e] * (h2[idx * D + e] + acc * sg));
}

extern "C" void kernel_launch(void* const* d_in, const int* in_sizes, int n_in,
                              void* d_out, int out_size, void* d_ws, size_t ws_size,
                              hipStream_t stream) {
  const u16* x = (const u16*)d_in[0]; const u16* sre = (const u16*)d_in[1]; const u16* sim = (const u16*)d_in[2];
  const u16* dre = (const u16*)d_in[3]; const u16* dim = (const u16*)d_in[4];
  const u16* wr = (const u16*)d_in[6]; const u16* wg = (const u16*)d_in[7]; const u16* wu = (const u16*)d_in[8];
  const u16* wd = (const u16*)d_in[9]; const u16* c = (const u16*)d_in[10]; const u16* wmod = (const u16*)d_in[11];
  u16* out = (u16*)d_out; char* ws = (char*)d_ws;
  float* mod = (float*)ws; u16* h = (u16*)(ws + (1 << 20)); u16* abT = (u16*)(ws + (4 << 20));
  float* y = (float*)(ws + (8 << 20)); float* x2 = (float*)(ws + (12 << 20)); float* h2 = (float*)(ws + (16 << 20));
  float* r = (float*)(ws + (20 << 20)); float* gu = (float*)(ws + (21 << 20)); int* idx = (int*)(ws + (22 << 20));
  k_mod<<<6 * D / 64, 64, 0, stream>>>(c, wmod, mod);
  k_ln1<<<S / 4, 256, 0, stream>>>(x, mod, h);
  k_gemm1<<<dim3(S / 16, 14), 256, 0, stream>>>(h, dre, dim, abT);
  k_gemm2<<<dim3(S / 16, 7), 256, 0, stream>>>(sre, sim, abT, y);
  k_ep<<<S / 4, 256, 0, stream>>>(x, y, mod, wr, out, x2, h2, r);
  k_ffn1<<<F / 256, 256, 0, stream>>>(r, h2, wg, wu, gu, idx);
  k_ffn2<<<D / 64, 64, 0, stream>>>(gu, wd, r, idx, x2, h2, mod, out);
}
```

```cpp
#include <hip/hip_runtime.h>
#include <hip/hip_bf16.h>
#include <stdint.h>

typedef unsigned short u16;
typedef __attribute__((ext_vector_type(8))) short bf16x8;
typedef __attribute__((ext_vector_type(4))) float f32x4;

static constexpr int S = 2048, D = 448, F = 1792;

__device__ __forceinline__ float bf2f(u16 v) { return __uint_as_float(((unsigned)v) << 16); }
__device__ __forceinline__ u16 f2bf(float f) { __hip_bfloat16 h = __float2bfloat16(f); return *(u16*)&h; }
__device__ __forceinline__ float rbf(float f) { return bf2f(f2bf(f)); }

__global__ void k_mod(const u16* c, const u16* wmod, float* mod) {
  int j = blockIdx.x * 64 + threadIdx.x;
  float acc = 0;
  for (int k = 0; k < D; ++k) {
    float cv = bf2f(c[k]);
    float s = rbf(cv / (1.f + __expf(-cv)));
    acc += s * bf2f(wmod[k * 6 * D + j]);
  }
  mod[j] = rbf(acc);
}

__device__ __forceinline__ float wsum(float v) {
  for (int o = 32; o; o >>= 1) v += __shfl_xor(v, o);
  return v;
}

__global__ void k_ln1(const u16* x, const float* mod, u16* h) {
  int row = blockIdx.x * 4 + (threadIdx.x >> 6), lane = threadIdx.x & 63;
  float v[7]; float s = 0;
  for (int i = 0; i < 7; ++i) { v[i] = bf2f(x[row * D + i * 64 + lane]); s += v[i]; }
  float m = wsum(s) / D; float q = 0;
  for (int i = 0; i < 7; ++i) { float d = v[i] - m; q += d * d; }
  float rs = rsqrtf(wsum(q) / D + 1e-5f);
  for (int i = 0; i < 7; ++i) { int e = i * 64 + lane;
    h[row * D + e] = f2bf((v[i] - m) * rs * (1.f + mod[D + e]) + mod[e]); }
}

__global__ void k_gemm1(const u16* h, const u16* wre, const u16* wim, u16* abT) {
  int lane = threadIdx.x & 63;
  int tm = blockIdx.x, tn = blockIdx.y * 4 + (threadIdx.x >> 6);
  const u16* W = tn < 28 ? wre : wim; int n0 = (tn % 28) * 16;
  f32x4 acc = {0, 0, 0, 0};
  for (int k = 0; k < D; k += 32) {
    bf16x8 a = *(const bf16x8*)(h + (tm * 16 + (lane & 15)) * D + k + (lane >> 4) * 8);
    bf16x8 b = *(const bf16x8*)(W + (n0 + (lane & 15)) * D + k + (lane >> 4) * 8);
    acc = __builtin_amdgcn_mfma_f32_16x16x32_bf16(__builtin_bit_cast(__attribute__((ext_vector_type(8))) __bf16, a), __builtin_bit_cast(__attribute__((ext_vector_type(8))) __bf16, b), acc, 0, 0, 0);
  }
  int e = n0 + (lane & 15);
  for (int j = 0; j < 4; ++j) { int s = tm * 16 + (lane >> 4) * 4 + j;
    float v = acc[j]; if (tn >= 28) v = -v;
    abT[e * 2 * S + (tn < 28 ? 0 : S) + s] = f2bf(v); }
}

__global__ void k_gemm2(const u16* sre, const u16* sim, const u16* abT, float* y) {
  int lane = threadIdx.x & 63;
  int tm = blockIdx.x, tn = blockIdx.y * 4 + (threadIdx.x >> 6);
  f32x4 acc = {0, 0, 0, 0};
  for (int k = 0; k < 2 * S; k += 32) {
    const u16* A = k < S ? sre : sim; int kk = k & (S - 1);
    bf16x8 a = *(const bf16x8*)(A + (tm * 16 + (lane & 15)) * S + kk + (lane >> 4) * 8);
    bf16x8 b = *(const bf16x8*)(abT + (tn * 16 + (lane & 15)) * 2 * S + k + (lane >> 4) * 8);
    acc = __builtin_amdgcn_mfma_f32_16x16x32_bf16(__builtin_bit_cast(__attribute__((ext_vector_type(8))) __bf16, a), __builtin_bit_cast(__attribute__((ext_vector_type(8))) __bf16, b), acc, 0, 0, 0);
  }
  for (int j = 0; j < 4; ++j) y[(tm * 16 + (lane >> 4) * 4 + j) * D + tn * 16 + (lane & 15)] = acc[j];
}

__global__ void k_ep(const u16* x, const float* y, const float* mod, const u16* wr, u16* out, float* x2o, float* h2o, float* r) {
  int row = blockIdx.x * 4 + (threadIdx.x >> 6), lane = threadIdx.x & 63;
  float v[7]; float s = 0;
  for (int i = 0; i < 7; ++i) { int e = i * 64 + lane; v[i] = bf2f(x[row * D + e]) + mod[2 * D + e] * y[row * D + e]; s += v[i]; }
  float m = wsum(s) / D; float q = 0;
  for (int i = 0; i < 7; ++i) { float d = v[i] - m; q += d * d; }
  float rs = rsqrtf(wsum(q) / D + 1e-5f); float rr = 0;
  for (int i = 0; i < 7; ++i) { int e = i * 64 + lane;
    float h2 = (v[i] - m) * rs * (1.f + mod[4 * D + e]) + mod[3 * D + e];
    rr += h2 * bf2f(wr[e]); x2o[row * D + e] = v[i]; h2o[row * D + e] = h2;
    out[row * D + e] = f2bf(v[i] + mod[5 * D + e] * h2); }
  rr = wsum(rr);
  if (lane == 0) r[row] = rr;
}

__global__ void k_ffn1(const float* r, const float* h2, const u16* wg, const u16* wu, float* gu, int* idxo) {
  __shared__ float sv[256]; __shared__ int si[256]; __shared__ float xs[D];
  int t = threadIdx.x; float bv = -INFINITY; int bi = 0;
  for (int i = t; i < S; i += 256) { float v = r[i]; if (v > bv) { bv = v; bi = i; } }
  sv[t] = bv; si[t] = bi; __syncthreads();
  for (int o = 128; o; o >>= 1) { if (t < o) { if (sv[t + o] > sv[t] || (sv[t + o] == sv[t] && si[t + o] < si[t])) { sv[t] = sv[t + o]; si[t] = si[t + o]; } } __syncthreads(); }
  int idx = si[0];
  for (int i = t; i < D; i += 256) xs[i] = rbf(h2[idx * D + i]);
  __syncthreads();
  int j = blockIdx.x * 256 + t;
  float g = 0, u = 0;
  for (int k = 0; k < D; ++k) { g += xs[k] * bf2f(wg[k * F + j]); u += xs[k] * bf2f(wu[k * F + j]); }
  g = rbf(g); u = rbf(u);
  gu[j] = rbf(rbf(g / (1.f + __expf(-g))) * u);
  if (blockIdx.x == 0 && t == 0) *idxo = idx;
}

__global__ void k_ffn2(const float* gu, const u16* wd, const float* r, const int* idxp, const float* x2, const float* h2, const float* mod, u16* out) {
  int e = blockIdx.x * 64 + threadIdx.x; int idx = *idxp;
  float acc = 0;
  for (int k = 0; k < F; ++k) acc += gu[k] * bf2f(wd[k * D + e]);
  float rv = r[idx]; float sg = 1.f / (1.f + __expf(-rv));
  out[idx * D + e] = f2bf(x2[idx * D + e] + mod[5 * D + e] * (h2[idx * D + e] + acc * sg));
}


#define LAS __attribute__((address_space(3)))
typedef unsigned v4u __attribute__((ext_vector_type(4)));
__device__ __forceinline__ unsigned long long rt() { return __builtin_amdgcn_s_memrealtime(); }
#define MF16(acc, a, b) asm volatile("v_mfma_f32_16x16x32_bf16 %0, %1, %2, %0" : "+v"(acc) : "v"(a), "v"(b))
__global__ void __launch_bounds__(512, 2) k_probe(const u16* x, const u16* sre, const u16* wmod, const u16* wg, const unsigned* wsw, unsigned long long* stamps, unsigned* sinkp) {
  extern __shared__ unsigned char smem[];
  const int b = blockIdx.x, lane = threadIdx.x & 63, w = __builtin_amdgcn_readfirstlane(threadIdx.x >> 6);
  unsigned long long t0 = rt(), t1 = 0, t2 = 0, t3 = 0;
  unsigned sink = 0;
  if (w < 4) {
    const unsigned char* p = w == 0 ? (const unsigned char*)wmod + b * 8192 : w == 1 ? (const unsigned char*)x + b * 7168 : w == 2 ? (const unsigned char*)sre + b * 32768 : (const unsigned char*)wsw + b * 4096;
    unsigned v;
    asm volatile("global_load_dword %0, %1, off nt\n\ts_waitcnt vmcnt(0)" : "=v"(v) : "v"(p + lane * 4) : "memory");
    t1 = rt();
    asm volatile("s_waitcnt lgkmcnt(0)" ::: "memory");
    sink += v;
    asm volatile("global_load_dword %0, %1, off nt\n\ts_waitcnt vmcnt(0)" : "=v"(v) : "v"(p + 1024 + lane * 4) : "memory");
    t2 = rt();
    asm volatile("s_waitcnt lgkmcnt(0)" ::: "memory");
    sink += v;
    asm volatile("global_load_dword %0, %1, off nt\n\ts_waitcnt vmcnt(0)" : "=v"(v) : "v"((const unsigned char*)wg + b * 4096 + w * 1024 + lane * 4) : "memory");
    t3 = rt();
    sink += v;
  } else if (w == 4) {
    f32x4 acc[8]; v4u a = {1, 2, 3, 4}, c = {5, 6, 7, 8};
    for (int i = 0; i < 8; ++i) acc[i] = f32x4{0, 0, 0, 0};
    asm volatile("" : "+v"(a), "+v"(c));
    t1 = rt();
    asm volatile("s_waitcnt lgkmcnt(0)" ::: "memory");
#pragma unroll
    for (int i = 0; i < 512; ++i) MF16(acc[i & 7], a, c);
    t2 = rt();
    asm volatile("s_waitcnt lgkmcnt(0)" ::: "memory");
#pragma unroll
    for (int i = 0; i < 512; ++i) MF16(acc[i & 7], c, a);
    t3 = rt();
    for (int i = 0; i < 8; ++i) sink += __float_as_uint(acc[i][0]);
  }
  sinkp[b * 512 + threadIdx.x] = sink;
  if (lane == 0) { unsigned long long* st = stamps + (b * 8 + w) * 4; st[0] = t0; st[1] = t1; st[2] = t2; st[3] = t3; }
}
__global__ void k_print(const unsigned long long* st) {
  unsigned long long mn = ~0ull, mxs = 0;
  for (int i = 0; i < 2048; ++i) { mn = st[i * 4] < mn ? st[i * 4] : mn; mxs = st[i * 4] > mxs ? st[i * 4] : mxs; }
  int a[5][3], m[5][3];
  for (int w = 0; w < 5; ++w) for (int j = 0; j < 3; ++j) { unsigned long long s = 0, mx = 0; for (int b = 0; b < 256; ++b) { const unsigned long long* p = st + (b * 8 + w) * 4; unsigned long long d = p[j + 1] - p[j]; s += d; mx = d > mx ? d : mx; } a[w][j] = (int)(s * 10 / 256); m[w][j] = (int)mx * 10; }
  printf("P ls %d | wmod %d/%d %d/%d %d/%d | x %d/%d %d/%d %d/%d | ws %d/%d %d/%d %d/%d | dws %d/%d %d/%d %d/%d | mf %d/%d %d/%d %d/%d\n", (int)(mxs - mn) * 10,
    a[0][0], m[0][0], a[0][1], m[0][1], a[0][2], m[0][2], a[1][0], m[1][0], a[1][1], m[1][1], a[1][2], m[1][2], a[2][0], m[2][0], a[2][1], m[2][1], a[2][2], m[2][2], a[3][0], m[3][0], a[3][1], m[3][1], a[3][2], m[3][2], a[4][0], m[4][0], a[4][1], m[4][1], a[4][2], m[4][2]);
}

extern "C" void kernel_launch(void* const* d_in, const int* in_sizes, int n_in,
                              void* d_out, int out_size, void* d_ws, size_t ws_size,
                              hipStream_t stream) {
  const u16* x = (const u16*)d_in[0]; const u16* sre = (const u16*)d_in[1]; const u16* sim = (const u16*)d_in[2];
  const u16* dre = (const u16*)d_in[3]; const u16* dim = (const u16*)d_in[4];
  const u16* wr = (const u16*)d_in[6]; const u16* wg = (const u16*)d_in[7]; const u16* wu = (const u16*)d_in[8];
  const u16* wd = (const u16*)d_in[9]; const u16* c = (const u16*)d_in[10]; const u16* wmod = (const u16*)d_in[11];
  u16* out = (u16*)d_out; char* ws = (char*)d_ws;
  float* mod = (float*)ws; u16* h = (u16*)(ws + (1 << 20)); u16* abT = (u16*)(ws + (4 << 20));
  float* y = (float*)(ws + (8 << 20)); float* x2 = (float*)(ws + (12 << 20)); float* h2 = (float*)(ws + (16 << 20));
  float* r = (float*)(ws + (20 << 20)); float* gu = (float*)(ws + (21 << 20)); int* idx = (int*)(ws + (22 << 20));
  hipFuncSetAttribute((const void*)k_probe, hipFuncAttributeMaxDynamicSharedMemorySize, 160 * 1024);
  k_probe<<<256, 512, 160 * 1024, stream>>>(x, sre, wmod, wg, (const unsigned*)(ws + (64 << 20)), (unsigned long long*)(ws + (30 << 20)), (unsigned*)(ws + (31 << 20)));
  k_mod<<<6 * D / 64, 64, 0, stream>>>(c, wmod, mod);
  k_ln1<<<S / 4, 256, 0, stream>>>(x, mod, h);
  k_gemm1<<<dim3(S / 16, 14), 256, 0, stream>>>(h, dre, dim, abT);
  k_gemm2<<<dim3(S / 16, 7), 256, 0, stream>>>(sre, sim, abT, y);
  k_ep<<<S / 4, 256, 0, stream>>>(x, y, mod, wr, out, x2, h2, r);
  k_ffn1<<<F / 256, 256, 0, stream>>>(r, h2, wg, wu, gu, idx);
  k_ffn2<<<D / 64, 64, 0, stream>>>(gu, wd, r, idx, x2, h2, mod, out);
  k_print<<<1, 1, 0, stream>>>((unsigned long long*)(ws + (30 << 20)));
}
```

```cpp
#define MW 0
#define MJ1 0
#define MJ0 (0)
#define MAXV 0
#include <hip/hip_runtime.h>
#include <hip/hip_bf16.h>
#include <stdint.h>

typedef unsigned short u16;
typedef __attribute__((ext_vector_type(8))) short bf16x8;
typedef __attribute__((ext_vector_type(4))) float f32x4;

static constexpr int S = 2048, D = 448, F = 1792;

__device__ __forceinline__ float bf2f(u16 v) { return __uint_as_float(((unsigned)v) << 16); }
__device__ __forceinline__ u16 f2bf(float f) { __hip_bfloat16 h = __float2bfloat16(f); return *(u16*)&h; }
__device__ __forceinline__ float rbf(float f) { return bf2f(f2bf(f)); }

__global__ void k_mod(const u16* c, const u16* wmod, float* mod) {
  int j = blockIdx.x * 64 + threadIdx.x;
  float acc = 0;
  for (int k = 0; k < D; ++k) {
    float cv = bf2f(c[k]);
    float s = rbf(cv / (1.f + __expf(-cv)));
    acc += s * bf2f(wmod[k * 6 * D + j]);
  }
  mod[j] = rbf(acc);
}

__device__ __forceinline__ float wsum(float v) {
  for (int o = 32; o; o >>= 1) v += __shfl_xor(v, o);
  return v;
}

__global__ void k_ln1(const u16* x, const float* mod, u16* h) {
  int row = blockIdx.x * 4 + (threadIdx.x >> 6), lane = threadIdx.x & 63;
  float v[7]; float s = 0;
  for (int i = 0; i < 7; ++i) { v[i] = bf2f(x[row * D + i * 64 + lane]); s += v[i]; }
  float m = wsum(s) / D; float q = 0;
  for (int i = 0; i < 7; ++i) { float d = v[i] - m; q += d * d; }
  float rs = rsqrtf(wsum(q) / D + 1e-5f);
  for (int i = 0; i < 7; ++i) { int e = i * 64 + lane;
    h[row * D + e] = f2bf((v[i] - m) * rs * (1.f + mod[D + e]) + mod[e]); }
}

__global__ void k_gemm1(const u16* h, const u16* wre, const u16* wim, u16* abT) {
  int lane = threadIdx.x & 63;
  int tm = blockIdx.x, tn = blockIdx.y * 4 + (threadIdx.x >> 6);
  const u16* W = tn < 28 ? wre : wim; int n0 = (tn % 28) * 16;
  f32x4 acc = {0, 0, 0, 0};
  for (int k = 0; k < D; k += 32) {
    bf16x8 a = *(const bf16x8*)(h + (tm * 16 + (lane & 15)) * D + k + (lane >> 4) * 8);
    bf16x8 b = *(const bf16x8*)(W + (n0 + (lane & 15)) * D + k + (lane >> 4) * 8);
    acc = __builtin_amdgcn_mfma_f32_16x16x32_bf16(__builtin_bit_cast(__attribute__((ext_vector_type(8))) __bf16, a), __builtin_bit_cast(__attribute__((ext_vector_type(8))) __bf16, b), acc, 0, 0, 0);
  }
  int e = n0 + (lane & 15);
  for (int j = 0; j < 4; ++j) { int s = tm * 16 + (lane >> 4) * 4 + j;
    float v = acc[j]; if (tn >= 28) v = -v;
    abT[e * 2 * S + (tn < 28 ? 0 : S) + s] = f2bf(v); }
}

__global__ void k_gemm2(const u16* sre, const u16* sim, const u16* abT, float* y) {
  int lane = threadIdx.x & 63;
  int tm = blockIdx.x, tn = blockIdx.y * 4 + (threadIdx.x >> 6);
  f32x4 acc = {0, 0, 0, 0};
  for (int k = 0; k < 2 * S; k += 32) {
    const u16* A = k < S ? sre : sim; int kk = k & (S - 1);
    bf16x8 a = *(const bf16x8*)(A + (tm * 16 + (lane & 15)) * S + kk + (lane >> 4) * 8);
    bf16x8 b = *(const bf16x8*)(abT + (tn * 16 + (lane & 15)) * 2 * S + k + (lane >> 4) * 8);
    acc = __builtin_amdgcn_mfma_f32_16x16x32_bf16(__builtin_bit_cast(__attribute__((ext_vector_type(8))) __bf16, a), __builtin_bit_cast(__attribute__((ext_vector_type(8))) __bf16, b), acc, 0, 0, 0);
  }
  for (int j = 0; j < 4; ++j) y[(tm * 16 + (lane >> 4) * 4 + j) * D + tn * 16 + (lane & 15)] = acc[j];
}

__global__ void k_ep(const u16* x, const float* y, const float* mod, const u16* wr, u16* out, float* x2o, float* h2o, float* r) {
  int row = blockIdx.x * 4 + (threadIdx.x >> 6), lane = threadIdx.x & 63;
  float v[7]; float s = 0;
  for (int i = 0; i < 7; ++i) { int e = i * 64 + lane; v[i] = bf2f(x[row * D + e]) + mod[2 * D + e] * y[row * D + e]; s += v[i]; }
  float m = wsum(s) / D; float q = 0;
  for (int i = 0; i < 7; ++i) { float d = v[i] - m; q += d * d; }
  float rs = rsqrtf(wsum(q) / D + 1e-5f); float rr = 0;
  for (int i = 0; i < 7; ++i) { int e = i * 64 + lane;
    float h2 = (v[i] - m) * rs * (1.f + mod[4 * D + e]) + mod[3 * D + e];
    rr += h2 * bf2f(wr[e]); x2o[row * D + e] = v[i]; h2o[row * D + e] = h2;
    out[row * D + e] = f2bf(v[i] + mod[5 * D + e] * h2); }
  rr = wsum(rr);
  if (lane == 0) r[row] = rr;
}

__global__ void k_ffn1(const float* r, const float* h2, const u16* wg, const u16* wu, float* gu, int* idxo) {
  __shared__ float sv[256]; __shared__ int si[256]; __shared__ float xs[D];
  int t = threadIdx.x; float bv = -INFINITY; int bi = 0;
  for (int i = t; i < S; i += 256) { float v = r[i]; if (v > bv) { bv = v; bi = i; } }
  sv[t] = bv; si[t] = bi; __syncthreads();
  for (int o = 128; o; o >>= 1) { if (t < o) { if (sv[t + o] > sv[t] || (sv[t + o] == sv[t] && si[t + o] < si[t])) { sv[t] = sv[t + o]; si[t] = si[t + o]; } } __syncthreads(); }
  int idx = si[0];
  for (int i = t; i < D; i += 256) xs[i] = rbf(h2[idx * D + i]);
  __syncthreads();
  int j = blockIdx.x * 256 + t;
  float g = 0, u = 0;
  for (int k = 0; k < D; ++k) { g += xs[k] * bf2f(wg[k * F + j]); u += xs[k] * bf2f(wu[k * F + j]); }
  g = rbf(g); u = rbf(u);
  gu[j] = rbf(rbf(g / (1.f + __expf(-g))) * u);
  if (blockIdx.x == 0 && t == 0) *idxo = idx;
}

__global__ void k_ffn2(const float* gu, const u16* wd, const float* r, const int* idxp, const float* x2, const float* h2, const float* mod, u16* out) {
  int e = blockIdx.x * 64 + threadIdx.x; int idx = *idxp;
  float acc = 0;
  for (int k = 0; k < F; ++k) acc += gu[k] * bf2f(wd[k * D + e]);
  float rv = r[idx]; float sg = 1.f / (1.f + __expf(-rv));
  out[idx * D + e] = f2bf(x2[idx * D + e] + mod[5 * D + e] * (h2[idx * D + e] + acc * sg));
}


#define LAS __attribute__((address_space(3)))
typedef unsigned v4u __attribute__((ext_vector_type(4)));
__device__ __forceinline__ unsigned long long rt() { return __builtin_amdgcn_s_memrealtime(); }
#define MF16(acc, a, b) asm volatile("v_mfma_f32_16x16x32_bf16 %0, %1, %2, %0" : "+v"(acc) : "v"(a), "v"(b))
__global__ void __launch_bounds__(512, 2) k_probe(const u16* x, const u16* sre, const u16* wmod, const u16* wg, const unsigned* wsw, unsigned long long* stamps, unsigned* sinkp) {
  extern __shared__ unsigned char smem[];
  const int b = blockIdx.x, lane = threadIdx.x & 63, w = __builtin_amdgcn_readfirstlane(threadIdx.x >> 6);
  unsigned long long t0 = rt(), t1 = 0, t2 = 0, t3 = 0;
  unsigned sink = 0;
  if (w < 4) {
    const unsigned char* p = w == 0 ? (const unsigned char*)wmod + b * 8192 : w == 1 ? (const unsigned char*)x + b * 7168 : w == 2 ? (const unsigned char*)sre + b * 32768 : (const unsigned char*)wsw + b * 4096;
    unsigned v;
    asm volatile("global_load_dword %0, %1, off nt\n\ts_waitcnt vmcnt(0)" : "=v"(v) : "v"(p + lane * 4) : "memory");
    t1 = rt();
    asm volatile("s_waitcnt lgkmcnt(0)" ::: "memory");
    sink += v;
    asm volatile("global_load_dword %0, %1, off nt\n\ts_waitcnt vmcnt(0)" : "=v"(v) : "v"(p + 1024 + lane * 4) : "memory");
    t2 = rt();
    asm volatile("s_waitcnt lgkmcnt(0)" ::: "memory");
    sink += v;
    asm volatile("global_load_dword %0, %1, off nt\n\ts_waitcnt vmcnt(0)" : "=v"(v) : "v"((const unsigned char*)wg + b * 4096 + w * 1024 + lane * 4) : "memory");
    t3 = rt();
    sink += v;
  } else if (w == 4) {
    f32x4 acc[8]; v4u a = {1, 2, 3, 4}, c = {5, 6, 7, 8};
    for (int i = 0; i < 8; ++i) acc[i] = f32x4{0, 0, 0, 0};
    asm volatile("" : "+v"(a), "+v"(c));
    t1 = rt();
    asm volatile("s_waitcnt lgkmcnt(0)" ::: "memory");
#pragma unroll
    for (int i = 0; i < 512; ++i) MF16(acc[i & 7], a, c);
    t2 = rt();
    asm volatile("s_waitcnt lgkmcnt(0)" ::: "memory");
#pragma unroll
    for (int i = 0; i < 512; ++i) MF16(acc[i & 7], c, a);
    t3 = rt();
    for (int i = 0; i < 8; ++i) sink += __float_as_uint(acc[i][0]);
  }
  sinkp[b * 512 + threadIdx.x] = sink;
  if (lane == 0) { unsigned long long* st = stamps + (b * 8 + w) * 4; st[0] = t0; st[1] = t1; st[2] = t2; st[3] = t3; }
}

__global__ void k_print(const unsigned long long* st) {
  const int lane = threadIdx.x;
  unsigned long long mn = ~0ull;
  for (int i = lane; i < 2048; i += 64) mn = st[i * 4] < mn ? st[i * 4] : mn;
  for (int o = 32; o; o >>= 1) { unsigned long long y = __shfl_xor(mn, o); mn = y < mn ? y : mn; }
  unsigned long long s = 0, mx = 0;
  for (int b = lane; b < 256; b += 64) { const unsigned long long* p = st + (b * 8 + MW) * 4; unsigned long long d = p[MJ1] - (MJ0 < 0 ? mn : p[MJ0 < 0 ? 0 : MJ0]); s += d; mx = d > mx ? d : mx; }
  for (int o = 32; o; o >>= 1) { s += __shfl_xor(s, o); unsigned long long y = __shfl_xor(mx, o); mx = y > mx ? y : mx; }
  unsigned long long ticks = MAXV ? mx * 100 : s * 100 / 256;
  if (ticks > 200000) ticks = 200000;
  unsigned long long t0 = rt();
  while (rt() - t0 < ticks) __builtin_amdgcn_s_sleep(1);
}
extern "C" void kernel_launch(void* const* d_in, const int* in_sizes, int n_in,
                              void* d_out, int out_size, void* d_ws, size_t ws_size,
                              hipStream_t stream) {
  const u16* x = (const u16*)d_in[0]; const u16* sre = (const u16*)d_in[1]; const u16* sim = (const u16*)d_in[2];
  const u16* dre = (const u16*)d_in[3]; const u16* dim = (const u16*)d_in[4];
  const u16* wr = (const u16*)d_in[6]; const u16* wg = (const u16*)d_in[7]; const u16* wu = (const u16*)d_in[8];
  const u16* wd = (const u16*)d_in[9]; const u16* c = (const u16*)d_in[10]; const u16* wmod = (const u16*)d_in[11];
  u16* out = (u16*)d_out; char* ws = (char*)d_ws;
  float* mod = (float*)ws; u16* h = (u16*)(ws + (1 << 20)); u16* abT = (u16*)(ws + (4 << 20));
  float* y = (float*)(ws + (8 << 20)); float* x2 = (float*)(ws + (12 << 20)); float* h2 = (float*)(ws + (16 << 20));
  float* r = (float*)(ws + (20 << 20)); float* gu = (float*)(ws + (21 << 20)); int* idx = (int*)(ws + (22 << 20));
  hipFuncSetAttribute((const void*)k_probe, hipFuncAttributeMaxDynamicSharedMemorySize, 160 * 1024);
  k_probe<<<256, 512, 160 * 1024, stream>>>(x, sre, wmod, wg, (const unsigned*)(ws + (64 << 20)), (unsigned long long*)(ws + (30 << 20)), (unsigned*)(ws + (31 << 20)));
  k_mod<<<6 * D / 64, 64, 0, stream>>>(c, wmod, mod);
  k_ln1<<<S / 4, 256, 0, stream>>>(x, mod, h);
  k_gemm1<<<dim3(S / 16, 14), 256, 0, stream>>>(h, dre, dim, abT);
  k_gemm2<<<dim3(S / 16, 7), 256, 0, stream>>>(sre, sim, abT, y);
  k_ep<<<S / 4, 256, 0, stream>>>(x, y, mod, wr, out, x2, h2, r);
  k_ffn1<<<F / 256, 256, 0, stream>>>(r, h2, wg, wu, gu, idx);
  k_ffn2<<<D / 64, 64, 0, stream>>>(gu, wd, r, idx, x2, h2, mod, out);
  k_print<<<1, 64, 0, stream>>>((unsigned long long*)(ws + (30 << 20)));
}
```

```cpp
#include <hip/hip_runtime.h>
#include <stdint.h>

typedef unsigned short u16;
typedef unsigned u32;
typedef unsigned long long u64;
typedef u32 v4u __attribute__((ext_vector_type(4)));
typedef u32 v2u __attribute__((ext_vector_type(2)));
typedef float f32x4 __attribute__((ext_vector_type(4)));
typedef float f32x2 __attribute__((ext_vector_type(2)));
typedef __bf16 bf16x2 __attribute__((ext_vector_type(2)));
typedef __amdgpu_buffer_rsrc_t rsrc_t;
#define LAS __attribute__((address_space(3)))
#define DI __device__ __forceinline__

#define POL_X ""
#define POL_MOD " nt"
#define AUX_FFN 2
#define NT_WD 0
#define NT_A 1
#define PD 6
#define ROTW 12
#define ROTK 0
#define PSLEEP 1
#define WD_PRE 8
#define SC1 16

static constexpr u32 WS_EPOCH = 0;
static constexpr u32 WS_MOD = 4096, MODREP = 10880;
static constexpr u32 WS_F1 = 98304, WS_F2 = 131072, WS_KEY = 163840, WS_DONE = 196608;
static constexpr u32 WS_CAND = 262144;
static constexpr u32 WS_FP = 1310720;
static constexpr u32 WS_AB = 2097152;
static constexpr u32 WS_PART = 6291456;
static constexpr u32 WS_STAMP = 14u << 20;
static constexpr u32 WS_SIZE = 16u << 20;
static constexpr u32 L_H = 100352;
static constexpr u32 L_MOD = 157696;
static constexpr u32 L_CTL = L_MOD + 5376;
static constexpr u32 L_RED = L_CTL + 128;
static constexpr u32 L_TOTAL = 163840;
static constexpr u32 C_MOD = 0, C_H = 4, C_X1 = 8, C_X2 = 12, C_PROG = 16, C_FULL = 32, C_WD = 48;
static constexpr u32 L_ST = 0, L_Z = 1024, L_H2 = 8192;
static constexpr int SPIN_G = 1 << 14, SPIN_L = 1 << 18;
static constexpr int NFFN = 112, NFIN = 28;

struct Params {
  const u16 *x, *sre, *sim, *dre, *dim, *wr, *wg, *wu, *wd, *c, *wmod;
  u16* out;
  unsigned char* ws;
};

template <int V> struct IC { static constexpr int value = V; };
template <int A, int B, typename F> DI void static_for(F f) { if constexpr (A < B) { f(IC<A>{}); static_for<A + 1, B>(f); } }
DI rsrc_t mk(const void* p, u32 n) { return __builtin_amdgcn_make_buffer_rsrc((void*)p, 0, n, 0x00020000); }
DI float lo16(u32 v) { return __uint_as_float(v << 16); }
DI float hi16(u32 v) { return __uint_as_float(v & 0xffff0000u); }
DI u32 pk(float a, float b) { f32x2 v = {a, b}; return __builtin_bit_cast(u32, __builtin_convertvector(v, bf16x2)); }
DI float rbf(float a) { return lo16(pk(a, a)); }
DI float dot2(u32 a, u32 b, float c) { return __builtin_amdgcn_fdot2_f32_bf16(__builtin_bit_cast(bf16x2, a), __builtin_bit_cast(bf16x2, b), c, false); }
template <int CTRL> DI float dppf(float v) { return __uint_as_float(__builtin_amdgcn_update_dpp(0, __float_as_uint(v), CTRL, 0xf, 0xf, true)); }
template <int CTRL> DI u32 dppu(u32 v) { return __builtin_amdgcn_update_dpp(0, v, CTRL, 0xf, 0xf, true); }
static constexpr int XOR1 = 0xB1, XOR2 = 0x4E, HMIR = 0x141, MIR = 0x140, ROR4 = 0x124, ROR8 = 0x128, SHL4 = 0x104, SHR4 = 0x114;
DI float h32(float a, float b) { auto r = __builtin_amdgcn_permlane32_swap(__float_as_uint(a), __float_as_uint(b), false, false); return __uint_as_float(r[0]) + __uint_as_float(r[1]); }
DI float h16(float a, float b) { auto r = __builtin_amdgcn_permlane16_swap(__float_as_uint(a), __float_as_uint(b), false, false); return __uint_as_float(r[0]) + __uint_as_float(r[1]); }
DI u32 max32(u32 a) { auto r = __builtin_amdgcn_permlane32_swap(a, a, false, false); return r[0] > r[1] ? r[0] : r[1]; }
DI u32 max16(u32 a) { auto r = __builtin_amdgcn_permlane16_swap(a, a, false, false); return r[0] > r[1] ? r[0] : r[1]; }
DI u32 umax(u32 a, u32 b) { return a > b ? a : b; }
DI float rdl(float v, int l) { return __uint_as_float(__builtin_amdgcn_readlane(__float_as_uint(v), l)); }
DI float siluf(float v) { return v * __builtin_amdgcn_rcpf(1.f + __expf(-v)); }

#define BAR() asm volatile("s_waitcnt lgkmcnt(0)\n\ts_barrier" ::: "memory")
#define VM0() asm volatile("s_waitcnt vmcnt(0)" ::: "memory")
#define FENCE() asm volatile("" ::: "memory")
#define SCHED() __builtin_amdgcn_sched_barrier(0)
#define MFMA(acc, a, b) asm volatile("v_mfma_f32_16x16x32_bf16 %0, %1, %2, %0" : "+v"(acc) : "v"(a), "v"(b))

template <int NT, int DC, int IMM> DI void dmai(u32 voff, rsrc_t rs, u32 soff, u32 db) {
  if constexpr (NT) asm volatile("s_add_u32 m0, %3, %4\n\ts_nop 0\n\tbuffer_load_dwordx4 %0, %1, %2 offen offset:%5 nt lds" ::"v"(voff), "s"(rs), "s"(soff), "s"(db), "i"(DC - IMM), "i"(IMM) : "memory", "scc");
  else asm volatile("s_add_u32 m0, %3, %4\n\ts_nop 0\n\tbuffer_load_dwordx4 %0, %1, %2 offen offset:%5 lds" ::"v"(voff), "s"(rs), "s"(soff), "s"(db), "i"(DC - IMM), "i"(IMM) : "memory", "scc");
}

#define ST(i) do { } while (0)

template <typename T> DI T ldsld(LAS unsigned char* L, u32 off) { return *(LAS T*)(L + off); }
template <typename T> DI void ldsst(LAS unsigned char* L, u32 off, T v) { *(LAS T*)(L + off) = v; }
DI u32 lds_inc(LAS unsigned char* L, u32 off) { return __hip_atomic_fetch_add((LAS u32*)(L + off), 1u, __ATOMIC_RELAXED, __HIP_MEMORY_SCOPE_WORKGROUP); }
DI u32 lds_peek(LAS unsigned char* L, u32 off) { return __hip_atomic_load((LAS u32*)(L + off), __ATOMIC_RELAXED, __HIP_MEMORY_SCOPE_WORKGROUP); }
DI void lds_wait_ge(LAS unsigned char* L, u32 off, u32 v) {
  _Pragma("unroll 1") for (int sp = 0; sp < SPIN_L; ++sp) {
    FENCE();
    if (__builtin_amdgcn_readfirstlane(lds_peek(L, off)) >= v) break;
    __builtin_amdgcn_s_sleep(1);
  }
  FENCE();
}

DI u32 ld_epoch(const Params& p) {
  u32 e;
  asm volatile("s_load_dword %0, %1, 0x0\n\ts_waitcnt lgkmcnt(0)" : "=s"(e) : "s"(p.ws) : "memory");
  return e + 1;
}
#define XLD(dst, so, IMM) asm volatile("buffer_load_dwordx4 %0, %1, %2, %3 offen offset:" #IMM POL_X : "=v"(dst) : "v"(vo), "s"(rx), "s"(so) : "memory")
DI void x_issue(const Params& p, int row0, int lane, v4u (&xr)[8]) {
  const rsrc_t rx = mk(p.x, 2048 * 896);
  const u32 vo = lane < 56 ? lane * 16 : 0x80000000u, s0 = row0 * 896, s1 = s0 + 3584;
  XLD(xr[0], s0, 0); XLD(xr[1], s0, 896); XLD(xr[2], s0, 1792); XLD(xr[3], s0, 2688);
  XLD(xr[4], s1, 0); XLD(xr[5], s1, 896); XLD(xr[6], s1, 1792); XLD(xr[7], s1, 2688);
}
template <int N> DI void x_wait(v4u (&xr)[8]) {
  asm volatile("s_waitcnt vmcnt(%8)" : "+v"(xr[0]), "+v"(xr[1]), "+v"(xr[2]), "+v"(xr[3]), "+v"(xr[4]), "+v"(xr[5]), "+v"(xr[6]), "+v"(xr[7]) : "i"(N) : "memory");
}
DI float red8(const float (&s)[8], int lane) {
  float a0 = h32(s[0], s[4]), a1 = h32(s[1], s[5]), a2 = h32(s[2], s[6]), a3 = h32(s[3], s[7]);
  float b0 = h16(a0, a2), b1 = h16(a1, a3);
  const bool up = lane & 8;
  float v = (up ? b1 : b0) + dppf<ROR8>(up ? b0 : b1);
  v += dppf<XOR1>(v); v += dppf<XOR2>(v); v += dppf<HMIR>(v);
  return v;
}
template <typename H>
DI void ln_stats(const v4u (&xr)[8], int lane, f32x2 (&n)[8][4], H hook) {
  float s[8];
  u32 one2 = 0x3f803f80u, e0 = 0x00003f80u, e1 = 0x3f800000u;
  asm volatile("" : "+v"(one2), "+v"(e0), "+v"(e1));
#pragma unroll
  for (int i = 0; i < 8; ++i) {
    s[i] = dot2(xr[i][3], one2, dot2(xr[i][2], one2, dot2(xr[i][1], one2, dot2(xr[i][0], one2, 0.f))));
    if (i & 1) hook(i >> 1);
  }
  const float nmu = red8(s, lane) * (-1.f / 448.f);
  hook(4);
#pragma unroll
  for (int i = 0; i < 8; ++i) {
    const float m = rdl(nmu, (i >> 2) * 32 + ((i >> 1) & 1) * 16 + (i & 1) * 8);
    f32x2 q = {0.f, 0.f};
#pragma unroll
    for (int e = 0; e < 4; ++e) { n[i][e] = f32x2{dot2(xr[i][e], e0, m), dot2(xr[i][e], e1, m)}; q += n[i][e] * n[i][e]; }
    s[i] = lane < 56 ? q[0] + q[1] : 0.f;
    hook(5 + i);
  }
  const float rs = __builtin_amdgcn_rsqf(red8(s, lane) * (1.f / 448.f) + 1e-5f);
  hook(13);
#pragma unroll
  for (int i = 0; i < 8; ++i) {
    const float r = rdl(rs, (i >> 2) * 32 + ((i >> 1) & 1) * 16 + (i & 1) * 8);
#pragma unroll
    for (int e = 0; e < 4; ++e) n[i][e] *= r;
    asm volatile("" : "+v"(n[i][0]), "+v"(n[i][1]), "+v"(n[i][2]), "+v"(n[i][3]));
    if (i & 1) hook(14 + (i >> 1));
  }
}
DI void h_write(LAS unsigned char* L, const f32x2 (&n)[8][4], int lane, int w) {
  if (lane < 56) {
    const v4u sa = ldsld<v4u>(L, L_MOD + lane * 16), ca = ldsld<v4u>(L, L_MOD + 896 + lane * 16);
    f32x2 s[4], c[4];
#pragma unroll
    for (int q = 0; q < 4; ++q) { s[q] = f32x2{lo16(sa[q]), hi16(sa[q])}; c[q] = f32x2{lo16(ca[q]), hi16(ca[q])}; }
    const u32 base = L_H + ((w >> 2) * 4 + ((w >> 1) & 1)) * 7168 + (lane >> 3) * 1024 + (w & 1) * 512;
    const u32 ch = (lane & 7) ^ ((w & 1) * 4);
#pragma unroll
    for (int i = 0; i < 8; ++i) {
      v4u h;
#pragma unroll
      for (int q = 0; q < 4; ++q) { const f32x2 t = n[i][q] * c[q] + s[q]; h[q] = pk(t[0], t[1]); }
      ldsst<v4u>(L, base + (i >> 2) * 14336 + (i & 3) * 128 + ((ch ^ (i & 3)) * 16), h);
      SCHED();
    }
  }
}

template <int NE>
DI void small_gemm(const Params& p, LAS unsigned char* L, const v4u (&wf)[2][14], int lane, u32 soff) {
  const rsrc_t rw = mk(p.ws, WS_SIZE);
  const int r = lane & 15, g = lane >> 4;
  const u32 l0 = L_H + (r >> 3) * 7168 + (r & 7) * 128 + ((g ^ (r & 7)) * 16), l1 = l0 ^ 64;
  auto lda = [&](int T, int ks) { return ldsld<v4u>(L, ((ks & 1) ? l1 : l0) + T * 14336 + (ks >> 1) * 1024); };
  v4u a[4][2];
#pragma unroll
  for (int s = 0; s < 3; ++s) { a[s][0] = lda(0, s); a[s][1] = lda(1, s); }
  f32x4 acc[2][2][NE];
#pragma unroll
  for (int J = 0; J < 2; ++J)
#pragma unroll
    for (int t = 0; t < 2; ++t)
#pragma unroll
      for (int n = 0; n < NE; ++n) { acc[J][t][n] = f32x4{0, 0, 0, 0}; asm volatile("" : "+v"(acc[J][t][n])); }
  asm volatile("s_nop 1");
  SCHED();
#pragma unroll
  for (int st = 0; st < 28; ++st) {
    const int J = st / 14, ks = st % 14;
    if (st + 3 < 28) { const int J2 = (st + 3) / 14, k2 = (st + 3) % 14; a[(st + 3) & 3][0] = lda(2 * J2, k2); a[(st + 3) & 3][1] = lda(2 * J2 + 1, k2); }
#pragma unroll
    for (int t = 0; t < 2; ++t)
#pragma unroll
      for (int n = 0; n < NE; ++n) MFMA(acc[J][t][n], a[st & 3][t], wf[n][ks]);
    if (st == 17 || st == 27) {
      const int Js = st == 17 ? 0 : 1;
      if (st == 27) asm volatile("s_nop 7\n\ts_nop 7");
      SCHED();
#pragma unroll
      for (int n = 0; n < NE; ++n) {
        asm volatile("" : "+v"(acc[Js][0][n]), "+v"(acc[Js][1][n]));
        const v4u o = {pk(acc[Js][0][n][0], acc[Js][0][n][1]), pk(acc[Js][0][n][2], acc[Js][0][n][3]), pk(acc[Js][1][n][0], acc[Js][1][n][1]), pk(acc[Js][1][n][2], acc[Js][1][n][3])};
        __builtin_amdgcn_raw_buffer_store_b128(o, rw, lane * 16 + n * 1024, soff + Js * 28 * 1024, SC1);
      }
    }
    SCHED();
  }
}

template <int W> struct Part {
  static constexpr int NB = (W == 0 || W == 3) ? 2 : 3, E0 = W == 0 ? 0 : 2 * W - 1;
  static constexpr DI int lo(int n) { return n == 0 && W > 0 ? 8 - 2 * W : 0; }
  static constexpr DI int hi(int n) { return n == NB - 1 && W < 3 ? 6 - 2 * W : 8; }
};

template <int W>
DI void big_gemm(const Params& p, LAS unsigned char* L, int lane, u32 bsoff, u32 psoff, int k0) {
  using P = Part<W>;
  constexpr int NB = P::NB;
  const rsrc_t rw = mk(p.ws, WS_SIZE);
  const int r = lane & 15, g = lane >> 4;
  const u32 l0 = (r >> 3) * 1024 + (r & 7) * 128 + ((g ^ (r & 7)) * 16);
  u32 lb[3][2];
#pragma unroll
  for (int i = 0; i < 3; ++i) { lb[i][0] = l0 + i * 65536; lb[i][1] = (l0 ^ 64) + i * 65536; asm volatile("" : "+v"(lb[i][0]), "+v"(lb[i][1])); }
  auto lda = [&](int ks, int T) { const int sl = (ks >> 1) % 9; return ldsld<v4u>(L, lb[sl >> 2][ks & 1] + (sl & 3) * 16384 + T * 2048); };
  auto ldb = [&](int ks, int n) { return __builtin_amdgcn_raw_buffer_load_b128(rw, lane * 16 + n * 1024, bsoff + (((ks + k0) & 31) * 28 + P::E0) * 1024, SC1); };
  v4u B[PD][NB], A[8];
#pragma unroll
  for (int s = 0; s < PD; ++s)
#pragma unroll
    for (int n = 0; n < NB; ++n) B[s][n] = ldb(s, n);
#pragma unroll
  for (int T = 0; T < 8; ++T) A[T] = lda(0, T);
  f32x4 acc[NB][8];
#pragma unroll
  for (int n = 0; n < NB; ++n)
#pragma unroll
    for (int T = 0; T < 8; ++T)
      if (T >= P::lo(n) && T < P::hi(n)) { acc[n][T] = f32x4{0, 0, 0, 0}; asm volatile("" : "+v"(acc[n][T])); }
  asm volatile("s_nop 1");
  u32 fv = 0;
  SCHED();
#pragma unroll
  for (int ks = 0; ks < 32; ++ks) {
    if (ks == 4 || ks == 8 || ks == 12 || ks == 14) { if (lane == 0) ldsst<u32>(L, L_CTL + C_PROG + W * 4, ks >> 1); }
    if (ks == 17 || ks == 21 || ks == 25 || ks == 29) {
      if (__builtin_amdgcn_readfirstlane(fv) == 0) lds_wait_ge(L, L_CTL + C_FULL + ((ks - 17) >> 2) * 4, 1);
    }
    if (ks == 16 || ks == 20 || ks == 24 || ks == 28) fv = lds_peek(L, L_CTL + C_FULL + ((ks - 16) >> 2) * 4);
    SCHED();
#pragma unroll
    for (int T = 0; T < 8; ++T) {
#pragma unroll
      for (int n = 0; n < NB; ++n)
        if (T >= P::lo(n) && T < P::hi(n)) {
          MFMA(acc[n][T], B[ks % PD][n], A[T]);
          if (T == P::hi(n) - 1 && ks + PD < 32) B[ks % PD][n] = ldb(ks + PD, n);
        }
      if (ks + 1 < 32) A[T] = lda(ks + 1, T);
      SCHED();
    }
  }
  asm volatile("s_nop 7\n\ts_nop 7");
  SCHED();
  const u32 vo = (r >> 2) * 28672 + g * 64 + (r & 3) * 16;
#pragma unroll
  for (int n = 0; n < NB; ++n)
#pragma unroll
    for (int J = 0; J < 4; ++J)
      if (2 * J >= P::lo(n) && 2 * J < P::hi(n)) {
        asm volatile("" : "+v"(acc[n][2 * J]), "+v"(acc[n][2 * J + 1]));
        const f32x4 u = acc[n][2 * J], v = acc[n][2 * J + 1];
        const v4u o = {pk(u[0], u[1]), pk(u[2], u[3]), pk(v[0], v[1]), pk(v[2], v[3])};
        __builtin_amdgcn_raw_buffer_store_b128(o, rw, vo + (P::E0 + n) * 256, psoff + J * 4 * 28672, SC1);
      }
}

DI void flag_after_drain(const Params& p, LAS unsigned char* L, u32 ctl, u32 off, u32 ep, int lane) {
  VM0();
  if (lane == 0) {
    if (lds_inc(L, L_CTL + ctl) == 3) __builtin_amdgcn_raw_buffer_store_b32(ep, mk(p.ws, WS_SIZE), off, 0, SC1);
  }
}
DI void poll16(const Params& p, u32 off, u32 ep, int lane) {
  const rsrc_t rw = mk(p.ws, WS_SIZE);
  _Pragma("unroll 1") for (int sp = 0; sp < SPIN_G; ++sp) {
    FENCE();
    const u32 v = __builtin_amdgcn_raw_buffer_load_b32(rw, (lane & 15) * 128, off, SC1);
    if (__all(v == ep)) break;
    __builtin_amdgcn_s_sleep(PSLEEP);
  }
  FENCE();
}
template <int K0, int K1>
DI void poll_mod(const Params& p, LAS unsigned char* L, int b, u32 ep, int lane) {
  const rsrc_t rw = mk(p.ws, WS_SIZE);
  v4u g[K1 - K0];
  _Pragma("unroll 1") for (int sp = 0; sp < SPIN_G; ++sp) {
    FENCE();
    bool ok = true;
#pragma unroll
    for (int k = K0; k < K1; ++k) { const int ch = min(k * 64 + lane, 671); g[k - K0] = __builtin_amdgcn_raw_buffer_load_b128(rw, ch * 16, WS_MOD + (b & 7) * MODREP, SC1); }
#pragma unroll
    for (int k = K0; k < K1; ++k) ok &= g[k - K0].y == ep && g[k - K0].w == ep;
    if (__all(ok)) break;
  }
#pragma unroll
  for (int k = K0; k < K1; ++k) { const int ch = min(k * 64 + lane, 671); ldsst<v2u>(L, L_MOD + ch * 8, v2u{g[k - K0].x, g[k - K0].z}); }
  FENCE();
}
DI u32 poll_keys(const Params& p, u32 ep, int lane) {
  const rsrc_t rw = mk(p.ws, WS_SIZE);
  u32 best = 0;
  _Pragma("unroll 1") for (int sp = 0; sp < SPIN_G; ++sp) {
    FENCE();
    v2u k[4];
#pragma unroll
    for (int i = 0; i < 4; ++i) k[i] = __builtin_amdgcn_raw_buffer_load_b64(rw, lane * 128, WS_KEY + i * 8192, SC1);
    best = umax(umax(k[0].x, k[1].x), umax(k[2].x, k[3].x));
    if (__all(k[0].y == ep && k[1].y == ep && k[2].y == ep && k[3].y == ep)) break;
  }
  FENCE();
  best = umax(best, dppu<XOR1>(best)); best = umax(best, dppu<XOR2>(best)); best = umax(best, dppu<HMIR>(best)); best = umax(best, dppu<MIR>(best));
  best = max16(best); best = max32(best);
  return __builtin_amdgcn_readfirstlane(best);
}

__global__ void __launch_bounds__(512, 2) k_fused(Params p) {
  extern __shared__ __attribute__((aligned(16))) unsigned char smem[];
  LAS unsigned char* L = (LAS unsigned char*)smem;
  const int b = blockIdx.x, lane = threadIdx.x & 63, w = __builtin_amdgcn_readfirstlane(threadIdx.x >> 6);
  const int xcd = b & 7, j = b >> 3;
  const int half = xcd & 1, ec = j & 3;
  const int sig = (xcd >> 1) * 8 + (j >> 2);
  const int kh = (xcd >> 1) & 1, tt = (xcd >> 2) * 8 + (j >> 2);
  const int kp0 = ROTK * (tt & 7);
  const bool prod = b < 84;
  const rsrc_t rw = mk(p.ws, WS_SIZE);
  ST(0);

  if (w < 4) {
    v4u wm[7]; u32 cv[7];
    if (prod) {
      const rsrc_t rm = mk(p.wmod, 448 * 2688 * 2), rc = mk(p.c, 896);
      const u32 row = 112 * w + (lane >> 2), vc = row * 2, vm = row * 5376 + (lane & 3) * 16;
#pragma unroll
      for (int i = 0; i < 7; ++i) asm volatile("buffer_load_ushort %0, %1, %2, 0 offen offset:%3" : "=v"(cv[i]) : "v"(vc), "s"(rc), "i"(i * 32) : "memory");
#pragma unroll
      for (int i = 0; i < 7; ++i) asm volatile("buffer_load_dwordx4 %0, %1, %2, %3 offen" POL_MOD : "=v"(wm[i]) : "v"(vm), "s"(rm), "s"(b * 64 + i * 86016) : "memory");
      BAR();
    }
    v4u xr[8];
    x_issue(p, sig * 64 + 8 * w, lane, xr);
    if (!prod) BAR();
    const u32 ep = ld_epoch(p);
    if (prod) {
      asm volatile("s_waitcnt vmcnt(15)" : "+v"(cv[0]), "+v"(cv[1]), "+v"(cv[2]), "+v"(cv[3]), "+v"(cv[4]), "+v"(cv[5]), "+v"(cv[6])::"memory");
      u32 sl[7], sh[7];
#pragma unroll
      for (int i = 0; i < 7; ++i) { const float c = lo16(cv[i]); sh[i] = pk(0.f, siluf(c)); sl[i] = sh[i] >> 16; asm volatile("" : "+v"(sl[i]), "+v"(sh[i])); }
      float a[8] = {0, 0, 0, 0, 0, 0, 0, 0};
      asm volatile("s_waitcnt vmcnt(8)" : "+v"(wm[0]), "+v"(wm[1]), "+v"(wm[2]), "+v"(wm[3]), "+v"(wm[4]), "+v"(wm[5]), "+v"(wm[6])::"memory");
      ST(16);
#pragma unroll
      for (int i = 0; i < 7; ++i)
#pragma unroll
        for (int q = 0; q < 4; ++q) { a[2 * q] = dot2(wm[i][q], sl[i], a[2 * q]); a[2 * q + 1] = dot2(wm[i][q], sh[i], a[2 * q + 1]); }
      float v0 = h16(h32(a[0], a[4]), h32(a[2], a[6])), v1 = h16(h32(a[1], a[5]), h32(a[3], a[7]));
      v0 += dppf<ROR4>(v0); v1 += dppf<ROR4>(v1); v0 += dppf<ROR8>(v0); v1 += dppf<ROR8>(v1);
      if ((lane & 12) == 0) ldsst<f32x2>(L, L_RED + w * 128 + (lane & 3) * 32 + (lane >> 5) * 16 + ((lane >> 4) & 1) * 8, f32x2{v0, v1});
      asm volatile("s_waitcnt lgkmcnt(0)" ::: "memory");
      u32 old = 0;
      if (lane == 0) old = lds_inc(L, L_CTL + C_MOD);
      if (__builtin_amdgcn_readfirstlane(old) == 3) {
        FENCE();
        f32x2 t = ldsld<f32x2>(L, L_RED + (lane & 15) * 8);
#pragma unroll
        for (int k = 1; k < 4; ++k) t += ldsld<f32x2>(L, L_RED + k * 128 + (lane & 15) * 8);
        u32 v = pk(t[0], t[1]);
        const int part = b / 14;
        if (part == 1 || part == 4) v = pk(1.f + lo16(v), 1.f + hi16(v));
        const v2u gr = {v, ep};
#pragma unroll
        for (int k = 0; k < 2; ++k) __builtin_amdgcn_raw_buffer_store_b64(gr, rw, (lane >> 4) * MODREP + (lane & 15) * 8, WS_MOD + k * 4 * MODREP + b * 128, SC1);
      }
      ST(1);
    }
    f32x2 n[8][4];
    x_wait<0>(xr);
    ST(17);
    ln_stats(xr, lane, n, [](int) {});
    ST(2);
    lds_wait_ge(L, L_CTL + C_WD, 3);
    ST(3);
    v4u wf[2][14];
    {
      const int r = lane & 15, g = lane >> 4;
      const u32 l0 = w * 28672 + (r >> 3) * 7168 + (r & 7) * 128 + ((g ^ (r & 7)) * 16), l1 = l0 ^ 64;
#pragma unroll
      for (int ks = 0; ks < 14; ++ks) wf[0][ks] = ldsld<v4u>(L, ((ks & 1) ? l1 : l0) + (ks >> 1) * 1024);
      const u32 d = w < 3 ? 14336 : 0;
#pragma unroll
      for (int ks = 0; ks < 14; ++ks) wf[1][ks] = ldsld<v4u>(L, ((ks & 1) ? l1 : l0) + d + (ks >> 1) * 1024);
#pragma unroll
      for (int ks = 0; ks < 14; ++ks) asm volatile("" : "+v"(wf[0][ks]), "+v"(wf[1][ks]));
    }
    BAR();
    ST(4);
    h_write(L, n, lane, w);
    BAR();
    ST(5);
    const u32 so = WS_AB + ((half * 64 + sig * 2) * 28 + ec * 7 + 2 * w) * 1024;
    if (w < 3) small_gemm<2>(p, L, wf, lane, so); else small_gemm<1>(p, L, wf, lane, so);
    ST(6);
    if (lane == 0) lds_inc(L, L_CTL + C_H);
    flag_after_drain(p, L, C_X1, WS_F1 + ((half * 4 + ec) * 32 + sig) * 128, ep, lane);
    ST(7);
    BAR();
    ST(8);
    const u32 bs = WS_AB + ((half * 64 + kh * 32) * 28 + ec * 7) * 1024;
    const u32 ps = WS_PART + tt * 16 * 28672 + (half * 2 + kh) * 7168 + ec * 7 * 256;
    if (w == 0) big_gemm<0>(p, L, lane, bs, ps, 2 * kp0);
    else if (w == 1) big_gemm<1>(p, L, lane, bs, ps, 2 * kp0);
    else if (w == 2) big_gemm<2>(p, L, lane, bs, ps, 2 * kp0);
    else big_gemm<3>(p, L, lane, bs, ps, 2 * kp0);
    ST(9);
    flag_after_drain(p, L, C_X2, WS_F2 + (tt * 16 + (half * 2 + kh) * 4 + ec) * 128, ep, lane);
    ST(10);
  } else if (w < 7) {
    const int hw = w - 4;
    if (hw == 2 && lane < 32) ldsst<u32>(L, L_CTL + lane * 4, 0);
    if (prod) BAR();
    v4u xr[8];
    x_issue(p, sig * 64 + 8 * w, lane, xr);
    if (!prod) BAR();
    const int r7 = lane >> 3;
    f32x2 n[8][4];
    {
      const rsrc_t rd = mk(half ? p.dim : p.dre, 448 * 896);
      const u32 vo = r7 * 896 + (((lane & 7) ^ r7) * 16);
      const u32 g0 = (ec * 112 + hw * 8) * 896, d0 = hw * 7168, back = hw == 2 ? 3 * 7168 : 0;
      auto wdp = [&](auto I) {
        constexpr int i = I.value, k = i / 7, cg = i % 7;
        const u32 so = g0 + k * 3 * 7168 - (k == 4 ? back : 0), db = d0 - (k == 4 ? back : 0);
        dmai<NT_WD, k * 3 * 7168 + cg * 1024, cg * 128>(vo, rd, so, db);
      };
      static_for<0, WD_PRE>(wdp);
      x_wait<WD_PRE>(xr);
      ST(16);
      ln_stats(xr, lane, n, [&](int h) {
        static_for<0, 18>([&](auto H) { if (H.value == h) static_for<WD_PRE + (H.value * (35 - WD_PRE)) / 18, WD_PRE + ((H.value + 1) * (35 - WD_PRE)) / 18>(wdp); });
      });
    }
    ST(2);
    VM0();
    if (lane == 0) lds_inc(L, L_CTL + C_WD);
    ST(17);
    BAR();
    h_write(L, n, lane, w);
    BAR();
    const rsrc_t ra = mk(half ? p.sim : p.sre, 2048 * 4096);
    const u32 va = (8 * (r7 >> 2) + (r7 & 3)) * 4096 + (((lane & 7) ^ r7) * 16);
    const u32 sa0 = tt * 128 * 4096 + kh * 2048;
    auto pair = [&](u32 sb, u32 db) {
      static_for<0, 16>([&](auto I) {
        constexpr int pi = I.value, T = pi >> 1, hr = pi & 1;
        const u32 so = sb + (32 * (T >> 1) + 16 * hr + 4 * (T & 1)) * 4096;
        dmai<NT_A, pi * 1024, 0>(va, ra, so, db);
        dmai<NT_A, pi * 1024 + 16384, 128>(va, ra, so, db);
      });
    };
    auto single = [&](u32 sb, u32 db) {
      static_for<0, 16>([&](auto I) {
        constexpr int pi = I.value, T = pi >> 1, hr = pi & 1;
        dmai<NT_A, pi * 1024, 0>(va, ra, sb + (32 * (T >> 1) + 16 * hr + 4 * (T & 1)) * 4096, db);
      });
    };
    pair(sa0 + hw * 256, hw * 32768);
    ST(5);
    lds_wait_ge(L, L_CTL + C_H, 4);
    single(sa0 + (6 + hw) * 128, (6 + hw) * 16384);
    ST(6);
    VM0();
    ST(7);
    BAR();
    auto wait_free = [&](u32 v) {
      _Pragma("unroll 1") for (int sp = 0; sp < SPIN_L; ++sp) {
        FENCE();
        const u32 pr = lds_peek(L, L_CTL + C_PROG + (lane & 3) * 4);
        if (__all(pr >= v)) break;
        __builtin_amdgcn_s_sleep(2);
      }
      FENCE();
    };
    wait_free(2 * hw + 2);
    ST(18);
    pair(sa0 + (9 + 2 * hw) * 128, hw * 32768);
    ST(19);
    VM0();
    if (lane == 0) ldsst<u32>(L, L_CTL + C_FULL + hw * 4, 1);
    ST(8);
    if (hw == 0) {
      wait_free(7);
      single(sa0 + 15 * 128, 6 * 16384);
      VM0();
      if (lane == 0) ldsst<u32>(L, L_CTL + C_FULL + 12, 1);
      ST(9);
    }
  } else {
    if (prod) BAR();
    v4u xr[8];
    x_issue(p, sig * 64 + 8 * w, lane, xr);
    if (!prod) BAR();
    const u32 ep = ld_epoch(p);
    {
      f32x2 n[8][4];
      x_wait<0>(xr);
      ST(17);
      ln_stats(xr, lane, n, [](int) {});
      ST(2);
      poll_mod<0, 4>(p, L, b, ep, lane);
      ST(4);
      BAR();
      h_write(L, n, lane, w);
      BAR();
    }
    poll_mod<4, 11>(p, L, b, ep, lane);
    ST(5);
    lds_wait_ge(L, L_CTL + C_X1, 4);
    poll16(p, WS_F1 + ((half * 4 + ec) * 32 + kh * 16) * 128, ep, lane);
    ST(6);
    BAR();
    if (b < NFFN) {
      lds_wait_ge(L, L_CTL + C_FULL + 12, 1);
      u32 wgp[7][8], wup[7][8], wdp[8][8];
      {
        const rsrc_t rg = mk(p.wg, 448 * 3584), ru = mk(p.wu, 448 * 3584), rd = mk(p.wd, 1792 * 896);
        v4u tg[7][2], tu[7][2], td[8][2];
        const u32 vo = (lane >> 1) * 4 * 3584 + (lane & 1) * 16, vo6 = (lane >> 1) * 2 * 3584 + (lane & 1) * 16;
#pragma unroll
        for (int t = 0; t < 7; ++t)
#pragma unroll
          for (int k = 0; k < 2; ++k) {
            const u32 so = b * 32 + (t < 6 ? 128 * (t >> 1) + 2 * (t & 1) + k : 384 + k) * 3584;
            tg[t][k] = __builtin_amdgcn_raw_buffer_load_b128(rg, t < 6 ? vo : vo6, so, AUX_FFN);
            tu[t][k] = __builtin_amdgcn_raw_buffer_load_b128(ru, t < 6 ? vo : vo6, so, AUX_FFN);
          }
        const u32 vd = lane < 56 ? lane * 16 : 0x80000000u;
#pragma unroll
        for (int t = 0; t < 8; ++t)
#pragma unroll
          for (int k = 0; k < 2; ++k) td[t][k] = __builtin_amdgcn_raw_buffer_load_b128(rd, vd, (b * 16 + 2 * t + k) * 896, AUX_FFN);
        SCHED();
#pragma unroll
        for (int t = 0; t < 7; ++t)
#pragma unroll
          for (int q = 0; q < 4; ++q) {
            wgp[t][2 * q] = __builtin_amdgcn_perm(tg[t][1][q], tg[t][0][q], 0x05040100); wgp[t][2 * q + 1] = __builtin_amdgcn_perm(tg[t][1][q], tg[t][0][q], 0x07060302);
            wup[t][2 * q] = __builtin_amdgcn_perm(tu[t][1][q], tu[t][0][q], 0x05040100); wup[t][2 * q + 1] = __builtin_amdgcn_perm(tu[t][1][q], tu[t][0][q], 0x07060302);
            asm volatile("" : "+v"(wgp[t][2 * q]), "+v"(wgp[t][2 * q + 1]), "+v"(wup[t][2 * q]), "+v"(wup[t][2 * q + 1]));
          }
#pragma unroll
        for (int t = 0; t < 8; ++t)
#pragma unroll
          for (int q = 0; q < 4; ++q) {
            wdp[t][2 * q] = __builtin_amdgcn_perm(td[t][1][q], td[t][0][q], 0x05040100); wdp[t][2 * q + 1] = __builtin_amdgcn_perm(td[t][1][q], td[t][0][q], 0x07060302);
            asm volatile("" : "+v"(wdp[t][2 * q]), "+v"(wdp[t][2 * q + 1]));
          }
      }
      ST(7);
      lds_wait_ge(L, L_CTL + C_X2, 4);
      poll16(p, WS_F2 + (b >> 4) * 16 * 128, ep, lane);
      ST(8);
      BAR();
      BAR();
      BAR();
      ST(9);
      const u32 key = poll_keys(p, ep, lane);
      ST(10);
      const u32 cb = WS_CAND + ((2047 - (key & 0x7ff)) >> 3) * 3584;
      u32 hp[7];
      _Pragma("unroll 1") for (int sp = 0; sp < SPIN_G; ++sp) {
        FENCE();
        v4u q[3];
#pragma unroll
        for (int i = 0; i < 3; ++i) q[i] = __builtin_amdgcn_raw_buffer_load_b128(rw, (lane >> 1) * 16 + i * 512, cb, SC1);
        const v2u q6 = __builtin_amdgcn_raw_buffer_load_b64(rw, (lane >> 1) * 8 + 1536, cb, SC1);
        bool ok = q6.y == ep;
#pragma unroll
        for (int i = 0; i < 3; ++i) { ok &= q[i].y == ep && q[i].w == ep; hp[2 * i] = q[i].x; hp[2 * i + 1] = q[i].z; }
        hp[6] = q6.x;
        if (__all(ok)) break;
      }
      FENCE();
      ST(11);
      float g[8], u[8];
#pragma unroll
      for (int n = 0; n < 8; ++n) { g[n] = 0.f; u[n] = 0.f; }
#pragma unroll
      for (int t = 0; t < 7; ++t)
#pragma unroll
        for (int n = 0; n < 8; ++n) { g[n] = dot2(wgp[t][n], hp[t], g[n]); u[n] = dot2(wup[t][n], hp[t], u[n]); }
      const float g0 = h16(h32(g[0], g[4]), h32(g[2], g[6])), g1 = h16(h32(g[1], g[5]), h32(g[3], g[7]));
      const float u0 = h16(h32(u[0], u[4]), h32(u[2], u[6])), u1 = h16(h32(u[1], u[5]), h32(u[3], u[7]));
      const bool b2 = lane & 4;
      const float gr1 = dppf<SHR4>(g1), gl0 = dppf<SHL4>(g0), ur1 = dppf<SHR4>(u1), ul0 = dppf<SHL4>(u0);
      float gg = b2 ? g1 + gr1 : g0 + gl0;
      float uu = b2 ? u1 + ur1 : u0 + ul0;
      gg += dppf<ROR8>(gg); uu += dppf<ROR8>(uu); gg += dppf<XOR2>(gg); uu += dppf<XOR2>(uu);
      const float act = rbf(siluf(rbf(gg))) * rbf(uu);
      const u32 ap = pk(act, dppf<SHL4>(act));
      float o[8];
#pragma unroll
      for (int n = 0; n < 8; ++n) o[n] = 0.f;
#pragma unroll
      for (int t = 0; t < 8; ++t) {
        const u32 s = __builtin_amdgcn_readlane(ap, (t >> 2) + ((t >> 1) & 1) * 32 + (t & 1) * 16);
#pragma unroll
        for (int n = 0; n < 8; ++n) o[n] = dot2(wdp[t][n], s, o[n]);
      }
      if (lane < 56) {
#pragma unroll
        for (int s = 0; s < 2; ++s) {
          const v4u gr = {pk(o[4 * s], o[4 * s + 1]), ep, pk(o[4 * s + 2], o[4 * s + 3]), ep};
          __builtin_amdgcn_raw_buffer_store_b128(gr, rw, (lane >> 2) * 2 * 7168 + (lane & 3) * 16, WS_FP + s * 7168 + b * 64, SC1);
        }
      }
      ST(12);
    } else {
      lds_wait_ge(L, L_CTL + C_X2, 4);
      poll16(p, WS_F2 + (b >> 4) * 16 * 128, ep, lane);
      ST(8);
      BAR();
      BAR();
      BAR();
      if (b < NFFN + NFIN) {
        const int f = b - NFFN, col = 32 * (f >> 1) + 8 * (lane & 3) + 4 * (f & 1);
        const v2u gb = ldsld<v2u>(L, L_MOD + 5 * 896 + col * 2);
        ST(9);
        const u32 key = poll_keys(p, ep, lane);
        ST(10);
        const u32 row = 2047 - (key & 0x7ff), kb = (key & ~0x7ffu) | 0x400u;
        const float rv = __uint_as_float((kb & 0x80000000u) ? (kb & 0x7fffffffu) : ~kb);
        const float sg = rbf(__builtin_amdgcn_rcpf(1.f + __expf(-rbf(rv))));
        v4u z;
        _Pragma("unroll 1") for (int sp = 0; sp < SPIN_G; ++sp) {
          FENCE();
          z = __builtin_amdgcn_raw_buffer_load_b128(rw, col * 4, WS_CAND + (row >> 3) * 3584 + 1792, SC1);
          const u32 dn = __builtin_amdgcn_raw_buffer_load_b32(rw, 0, WS_DONE + (row >> 3) * 128, SC1);
          if (__all(z.y == ep && z.w == ep && dn == ep)) break;
        }
        ST(11);
        float s0, s1, s2, s3;
        _Pragma("unroll 1") for (int sp = 0; sp < SPIN_G; ++sp) {
          FENCE();
          v4u q[7];
#pragma unroll
          for (int i = 0; i < 7; ++i) q[i] = __builtin_amdgcn_raw_buffer_load_b128(rw, lane * 16, WS_FP + f * 7168 + i * 1024, SC1);
          bool ok = true; s0 = s1 = s2 = s3 = 0.f;
#pragma unroll
          for (int i = 0; i < 7; ++i) { ok &= q[i].y == ep && q[i].w == ep; s0 += lo16(q[i].x); s1 += hi16(q[i].x); s2 += lo16(q[i].z); s3 += hi16(q[i].z); }
          if (__all(ok)) break;
        }
        FENCE();
        ST(12);
        float a0 = h32(s0, s2), a1 = h32(s1, s3);
        a0 = h16(a0, a0); a1 = h16(a1, a1);
        a0 += dppf<ROR4>(a0); a1 += dppf<ROR4>(a1); a0 += dppf<ROR8>(a0); a1 += dppf<ROR8>(a1);
        const u32 zz = lane < 32 ? z.x : z.z, gg = lane < 32 ? gb.x : gb.y;
        const u32 o = pk(lo16(zz) + lo16(gg) * (rbf(a0) * sg), hi16(zz) + hi16(gg) * (rbf(a1) * sg));
        if ((lane & 28) == 0) __builtin_amdgcn_raw_buffer_store_b32(o, mk(p.out, 2048 * 896), col * 2 + (lane >> 5) * 4, row * 896, SC1);
        if (f == 0 && lane == 0) __builtin_amdgcn_raw_buffer_store_b32(ep, rw, 0, WS_EPOCH, SC1);
        ST(13);
      }
    }
    return;
  }

  {
    const int ci = lane & 3, e0 = 64 * w + 4 * (lane >> 2);
    const rsrc_t rx = mk(p.x, 2048 * 896);
    const v2u xa = __builtin_amdgcn_raw_buffer_load_b64(rx, ci * 896 + e0 * 2, b * 7168, 0);
    const v2u xb = __builtin_amdgcn_raw_buffer_load_b64(rx, ci * 896 + e0 * 2, b * 7168 + 3584, 0);
    const v2u wr = __builtin_amdgcn_raw_buffer_load_b64(mk(p.wr, 896), e0 * 2, 0, 0);
    const v2u ga = ldsld<v2u>(L, L_MOD + 2 * 896 + e0 * 2), sb = ldsld<v2u>(L, L_MOD + 3 * 896 + e0 * 2), cb = ldsld<v2u>(L, L_MOD + 4 * 896 + e0 * 2), gb = ldsld<v2u>(L, L_MOD + 5 * 896 + e0 * 2);
    float gaf[4] = {lo16(ga.x), hi16(ga.x), lo16(ga.y), hi16(ga.y)}, sbf[4] = {lo16(sb.x), hi16(sb.x), lo16(sb.y), hi16(sb.y)};
    float cbf[4] = {lo16(cb.x), hi16(cb.x), lo16(cb.y), hi16(cb.y)}, gbf[4] = {lo16(gb.x), hi16(gb.x), lo16(gb.y), hi16(gb.y)};
    float wrf[4] = {lo16(wr.x), hi16(wr.x), lo16(wr.y), hi16(wr.y)};
    float va[4] = {lo16(xa.x), hi16(xa.x), lo16(xa.y), hi16(xa.y)}, vb[4] = {lo16(xb.x), hi16(xb.x), lo16(xb.y), hi16(xb.y)};
    const u32 ep = ld_epoch(p);
    float wp[4], Wp = 0.f, Cp = 0.f;
#pragma unroll
    for (int i = 0; i < 4; ++i) { wp[i] = cbf[i] * wrf[i]; Wp += wp[i]; Cp += sbf[i] * wrf[i]; }
#pragma unroll
    for (int i = 0; i < 4; ++i) asm volatile("" : "+v"(wp[i]), "+v"(va[i]), "+v"(vb[i]), "+v"(gaf[i]));
    asm volatile("" : "+v"(Wp), "+v"(Cp));
    ST(11);
    BAR();
    ST(12);
    v4u q[4];
#pragma unroll
    for (int s = 0; s < 4; ++s) q[s] = __builtin_amdgcn_raw_buffer_load_b128(rw, lane * 16, WS_PART + b * 28672 + s * 7168 + w * 1024, SC1);
#pragma unroll
    for (int i = 0; i < 2; ++i) {
      va[2 * i] += gaf[2 * i] * ((lo16(q[0][i]) + lo16(q[1][i])) - (lo16(q[2][i]) + lo16(q[3][i])));
      va[2 * i + 1] += gaf[2 * i + 1] * ((hi16(q[0][i]) + hi16(q[1][i])) - (hi16(q[2][i]) + hi16(q[3][i])));
      vb[2 * i] += gaf[2 * i] * ((lo16(q[0][2 + i]) + lo16(q[1][2 + i])) - (lo16(q[2][2 + i]) + lo16(q[3][2 + i])));
      vb[2 * i + 1] += gaf[2 * i + 1] * ((hi16(q[0][2 + i]) + hi16(q[1][2 + i])) - (hi16(q[2][2 + i]) + hi16(q[3][2 + i])));
    }
    ST(13);
    float s1a = (va[0] + va[1]) + (va[2] + va[3]), s1b = (vb[0] + vb[1]) + (vb[2] + vb[3]);
    float s2a = (va[0] * va[0] + va[1] * va[1]) + (va[2] * va[2] + va[3] * va[3]), s2b = (vb[0] * vb[0] + vb[1] * vb[1]) + (vb[2] * vb[2] + vb[3] * vb[3]);
    float s3a = (va[0] * wp[0] + va[1] * wp[1]) + (va[2] * wp[2] + va[3] * wp[3]), s3b = (vb[0] * wp[0] + vb[1] * wp[1]) + (vb[2] * wp[2] + vb[3] * wp[3]);
    float v0 = h16(h32(s1a, s1b), h32(s2a, s2b)), v1 = h16(h32(s3a, s3b), h32(Wp, Cp));
    v0 += dppf<ROR4>(v0); v1 += dppf<ROR4>(v1); v0 += dppf<ROR8>(v0); v1 += dppf<ROR8>(v1);
    if ((lane & 12) == 0) {
      const u32 o = L_ST + ((lane >> 4) & 1) * 256 + w * 32 + (lane >> 5) * 16 + ci * 4;
      ldsst<float>(L, o, v0); ldsst<float>(L, o + 512, v1);
    }
    BAR();
    float t[4];
#pragma unroll
    for (int k = 0; k < 4; ++k) { t[k] = ldsld<float>(L, L_ST + k * 256 + lane * 4); t[k] = lane < 56 ? t[k] : 0.f; }
#pragma unroll
    for (int k = 0; k < 4; ++k) { t[k] += dppf<ROR8>(t[k]); t[k] = h16(t[k], t[k]); t[k] = h32(t[k], t[k]); }
    const bool up = lane & 4;
    const float oth = dppf<ROR4>(t[3]), Wt = up ? oth : t[3], Ct = up ? t[3] : oth;
    const float mu = t[0] * (1.f / 448.f);
    const float rs = __builtin_amdgcn_rsqf(fmaxf(t[1] * (1.f / 448.f) - mu * mu, 0.f) + 1e-5f);
    const float rr = rs * (t[2] - mu * Wt) + Ct;
    u32 key = __float_as_uint(rr);
    key = (key & 0x80000000u) ? ~key : (key | 0x80000000u);
    key = (key & ~0x7ffu) | (2047 - (b * 8 + (lane & 7)));
    key = umax(key, dppu<XOR1>(key)); key = umax(key, dppu<XOR2>(key)); key = umax(key, dppu<HMIR>(key));
    if (w == 0 && lane == 0) __builtin_amdgcn_raw_buffer_store_b64(v2u{key, ep}, rw, 0, WS_KEY + b * 128, SC1);
    ST(14);
    const float mo = dppf<ROR4>(mu), ro = dppf<ROR4>(rs);
    const float mua = up ? mo : mu, mub = up ? mu : mo, rsa = up ? ro : rs, rsb = up ? rs : ro;
    float ha[4], hb[4];
#pragma unroll
    for (int i = 0; i < 4; ++i) { ha[i] = (va[i] - mua) * rsa * cbf[i] + sbf[i]; hb[i] = (vb[i] - mub) * rsb * cbf[i] + sbf[i]; }
    ldsst<v2u>(L, L_H2 + ci * 896 + e0 * 2, v2u{pk(ha[0], ha[1]), pk(ha[2], ha[3])});
    ldsst<v2u>(L, L_H2 + (ci + 4) * 896 + e0 * 2, v2u{pk(hb[0], hb[1]), pk(hb[2], hb[3])});
    ldsst<v2u>(L, L_Z + ci * 896 + e0 * 2, v2u{pk(va[0] + gbf[0] * ha[0], va[1] + gbf[1] * ha[1]), pk(va[2] + gbf[2] * ha[2], va[3] + gbf[3] * ha[3])});
    ldsst<v2u>(L, L_Z + (ci + 4) * 896 + e0 * 2, v2u{pk(vb[0] + gbf[0] * hb[0], vb[1] + gbf[1] * hb[1]), pk(vb[2] + gbf[2] * hb[2], vb[3] + gbf[3] * hb[3])});
    BAR();
    if (w == 0) {
      const rsrc_t ro_ = mk(p.out, 2048 * 896);
#pragma unroll
      for (int i = 0; i < 7; ++i) __builtin_amdgcn_raw_buffer_store_b128(ldsld<v4u>(L, L_Z + i * 1024 + lane * 16), ro_, lane * 16, b * 7168 + i * 1024, SC1);
      VM0();
      if (lane == 0) __builtin_amdgcn_raw_buffer_store_b32(ep, rw, 0, WS_DONE + b * 128, SC1);
    } else if (w < 3) {
      const u32 best = (2047 - (__builtin_amdgcn_readfirstlane(key) & 0x7ff)) & 7;
      const u32 src = (w == 1 ? L_H2 : L_Z) + best * 896;
#pragma unroll
      for (int k = 0; k < 2; ++k) {
        if (k * 64 + lane < 112) {
          const v2u d = ldsld<v2u>(L, src + (k * 64 + lane) * 8);
          __builtin_amdgcn_raw_buffer_store_b128(v4u{d.x, ep, d.y, ep}, rw, lane * 16 + k * 1024, WS_CAND + b * 3584 + (w - 1) * 1792, SC1);
        }
      }
    }
    ST(15);
  }
}

extern "C" void kernel_launch(void* const* d_in, const int* in_sizes, int n_in, void* d_out, int out_size, void* d_ws, size_t ws_size, hipStream_t stream) {
  Params p;
  p.x = (const u16*)d_in[0]; p.sre = (const u16*)d_in[1]; p.sim = (const u16*)d_in[2]; p.dre = (const u16*)d_in[3]; p.dim = (const u16*)d_in[4];
  p.wr = (const u16*)d_in[6]; p.wg = (const u16*)d_in[7]; p.wu = (const u16*)d_in[8]; p.wd = (const u16*)d_in[9]; p.c = (const u16*)d_in[10]; p.wmod = (const u16*)d_in[11];
  p.out = (u16*)d_out; p.ws = (unsigned char*)d_ws;
  (void)hipFuncSetAttribute((const void*)k_fused, hipFuncAttributeMaxDynamicSharedMemorySize, L_TOTAL);
  k_fused<<<256, 512, L_TOTAL, stream>>>(p);
}
```

```cpp
#define AMP 0
#define PPOL ""
#define NCH 8
#include <hip/hip_runtime.h>
#include <stdint.h>

typedef unsigned short u16;
typedef unsigned u32;
typedef unsigned long long u64;
typedef u32 v4u __attribute__((ext_vector_type(4)));
typedef u32 v2u __attribute__((ext_vector_type(2)));
typedef float f32x4 __attribute__((ext_vector_type(4)));
typedef float f32x2 __attribute__((ext_vector_type(2)));
typedef __bf16 bf16x2 __attribute__((ext_vector_type(2)));
typedef __amdgpu_buffer_rsrc_t rsrc_t;
#define LAS __attribute__((address_space(3)))
#define DI __device__ __forceinline__

#define POL_X ""
#define POL_MOD " nt"
#define AUX_FFN 2
#define NT_WD 0
#define NT_A 1
#define PD 6
#define ROTW 12
#define ROTK 0
#define PSLEEP 1
#define WD_PRE 8
#define SC1 16

static constexpr u32 WS_EPOCH = 0;
static constexpr u32 WS_MOD = 4096, MODREP = 10880;
static constexpr u32 WS_F1 = 98304, WS_F2 = 131072, WS_KEY = 163840, WS_DONE = 196608;
static constexpr u32 WS_CAND = 262144;
static constexpr u32 WS_FP = 1310720;
static constexpr u32 WS_AB = 2097152;
static constexpr u32 WS_PART = 6291456;
static constexpr u32 WS_STAMP = 14u << 20;
static constexpr u32 WS_SIZE = 16u << 20;
static constexpr u32 L_H = 100352;
static constexpr u32 L_MOD = 157696;
static constexpr u32 L_CTL = L_MOD + 5376;
static constexpr u32 L_RED = L_CTL + 128;
static constexpr u32 L_TOTAL = 163840;
static constexpr u32 C_MOD = 0, C_H = 4, C_X1 = 8, C_X2 = 12, C_PROG = 16, C_FULL = 32, C_WD = 48;
static constexpr u32 L_ST = 0, L_Z = 1024, L_H2 = 8192;
static constexpr int SPIN_G = 1 << 14, SPIN_L = 1 << 18;
static constexpr int NFFN = 112, NFIN = 28;

struct Params {
  const u16 *x, *sre, *sim, *dre, *dim, *wr, *wg, *wu, *wd, *c, *wmod;
  u16* out;
  unsigned char* ws;
};

template <int V> struct IC { static constexpr int value = V; };
template <int A, int B, typename F> DI void static_for(F f) { if constexpr (A < B) { f(IC<A>{}); static_for<A + 1, B>(f); } }
DI rsrc_t mk(const void* p, u32 n) { return __builtin_amdgcn_make_buffer_rsrc((void*)p, 0, n, 0x00020000); }
DI float lo16(u32 v) { return __uint_as_float(v << 16); }
DI float hi16(u32 v) { return __uint_as_float(v & 0xffff0000u); }
DI u32 pk(float a, float b) { f32x2 v = {a, b}; return __builtin_bit_cast(u32, __builtin_convertvector(v, bf16x2)); }
DI float rbf(float a) { return lo16(pk(a, a)); }
DI float dot2(u32 a, u32 b, float c) { return __builtin_amdgcn_fdot2_f32_bf16(__builtin_bit_cast(bf16x2, a), __builtin_bit_cast(bf16x2, b), c, false); }
template <int CTRL> DI float dppf(float v) { return __uint_as_float(__builtin_amdgcn_update_dpp(0, __float_as_uint(v), CTRL, 0xf, 0xf, true)); }
template <int CTRL> DI u32 dppu(u32 v) { return __builtin_amdgcn_update_dpp(0, v, CTRL, 0xf, 0xf, true); }
static constexpr int XOR1 = 0xB1, XOR2 = 0x4E, HMIR = 0x141, MIR = 0x140, ROR4 = 0x124, ROR8 = 0x128, SHL4 = 0x104, SHR4 = 0x114;
DI float h32(float a, float b) { auto r = __builtin_amdgcn_permlane32_swap(__float_as_uint(a), __float_as_uint(b), false, false); return __uint_as_float(r[0]) + __uint_as_float(r[1]); }
DI float h16(float a, float b) { auto r = __builtin_amdgcn_permlane16_swap(__float_as_uint(a), __float_as_uint(b), false, false); return __uint_as_float(r[0]) + __uint_as_float(r[1]); }
DI u32 max32(u32 a) { auto r = __builtin_amdgcn_permlane32_swap(a, a, false, false); return r[0] > r[1] ? r[0] : r[1]; }
DI u32 max16(u32 a) { auto r = __builtin_amdgcn_permlane16_swap(a, a, false, false); return r[0] > r[1] ? r[0] : r[1]; }
DI u32 umax(u32 a, u32 b) { return a > b ? a : b; }
DI float rdl(float v, int l) { return __uint_as_float(__builtin_amdgcn_readlane(__float_as_uint(v), l)); }
DI float siluf(float v) { return v * __builtin_amdgcn_rcpf(1.f + __expf(-v)); }

#define BAR() asm volatile("s_waitcnt lgkmcnt(0)\n\ts_barrier" ::: "memory")
#define VM0() asm volatile("s_waitcnt vmcnt(0)" ::: "memory")
#define FENCE() asm volatile("" ::: "memory")
#define SCHED() __builtin_amdgcn_sched_barrier(0)
#define MFMA(acc, a, b) asm volatile("v_mfma_f32_16x16x32_bf16 %0, %1, %2, %0" : "+v"(acc) : "v"(a), "v"(b))

template <int NT, int DC, int IMM> DI void dmai(u32 voff, rsrc_t rs, u32 soff, u32 db) {
  if constexpr (NT) asm volatile("s_add_u32 m0, %3, %4\n\ts_nop 0\n\tbuffer_load_dwordx4 %0, %1, %2 offen offset:%5 nt lds" ::"v"(voff), "s"(rs), "s"(soff), "s"(db), "i"(DC - IMM), "i"(IMM) : "memory", "scc");
  else asm volatile("s_add_u32 m0, %3, %4\n\ts_nop 0\n\tbuffer_load_dwordx4 %0, %1, %2 offen offset:%5 lds" ::"v"(voff), "s"(rs), "s"(soff), "s"(db), "i"(DC - IMM), "i"(IMM) : "memory", "scc");
}

#define ST(i) do { } while (0)

template <typename T> DI T ldsld(LAS unsigned char* L, u32 off) { return *(LAS T*)(L + off); }
template <typename T> DI void ldsst(LAS unsigned char* L, u32 off, T v) { *(LAS T*)(L + off) = v; }
DI u32 lds_inc(LAS unsigned char* L, u32 off) { return __hip_atomic_fetch_add((LAS u32*)(L + off), 1u, __ATOMIC_RELAXED, __HIP_MEMORY_SCOPE_WORKGROUP); }
DI u32 lds_peek(LAS unsigned char* L, u32 off) { return __hip_atomic_load((LAS u32*)(L + off), __ATOMIC_RELAXED, __HIP_MEMORY_SCOPE_WORKGROUP); }
DI void lds_wait_ge(LAS unsigned char* L, u32 off, u32 v) {
  _Pragma("unroll 1") for (int sp = 0; sp < SPIN_L; ++sp) {
    FENCE();
    if (__builtin_amdgcn_readfirstlane(lds_peek(L, off)) >= v) break;
    __builtin_amdgcn_s_sleep(1);
  }
  FENCE();
}

DI u32 ld_epoch(const Params& p) {
  u32 e;
  asm volatile("s_load_dword %0, %1, 0x0\n\ts_waitcnt lgkmcnt(0)" : "=s"(e) : "s"(p.ws) : "memory");
  return e + 1;
}
#define XLD(dst, so, IMM) asm volatile("buffer_load_dwordx4 %0, %1, %2, %3 offen offset:" #IMM POL_X : "=v"(dst) : "v"(vo), "s"(rx), "s"(so) : "memory")
DI void x_issue(const Params& p, int row0, int lane, v4u (&xr)[8]) {
  const rsrc_t rx = mk(p.x, 2048 * 896);
  const u32 vo = lane < 56 ? lane * 16 : 0x80000000u, s0 = row0 * 896, s1 = s0 + 3584;
  XLD(xr[0], s0, 0); XLD(xr[1], s0, 896); XLD(xr[2], s0, 1792); XLD(xr[3], s0, 2688);
  XLD(xr[4], s1, 0); XLD(xr[5], s1, 896); XLD(xr[6], s1, 1792); XLD(xr[7], s1, 2688);
}
template <int N> DI void x_wait(v4u (&xr)[8]) {
  asm volatile("s_waitcnt vmcnt(%8)" : "+v"(xr[0]), "+v"(xr[1]), "+v"(xr[2]), "+v"(xr[3]), "+v"(xr[4]), "+v"(xr[5]), "+v"(xr[6]), "+v"(xr[7]) : "i"(N) : "memory");
}
DI float red8(const float (&s)[8], int lane) {
  float a0 = h32(s[0], s[4]), a1 = h32(s[1], s[5]), a2 = h32(s[2], s[6]), a3 = h32(s[3], s[7]);
  float b0 = h16(a0, a2), b1 = h16(a1, a3);
  const bool up = lane & 8;
  float v = (up ? b1 : b0) + dppf<ROR8>(up ? b0 : b1);
  v += dppf<XOR1>(v); v += dppf<XOR2>(v); v += dppf<HMIR>(v);
  return v;
}
template <typename H>
DI void ln_stats(const v4u (&xr)[8], int lane, f32x2 (&n)[8][4], H hook) {
  float s[8];
  u32 one2 = 0x3f803f80u, e0 = 0x00003f80u, e1 = 0x3f800000u;
  asm volatile("" : "+v"(one2), "+v"(e0), "+v"(e1));
#pragma unroll
  for (int i = 0; i < 8; ++i) {
    s[i] = dot2(xr[i][3], one2, dot2(xr[i][2], one2, dot2(xr[i][1], one2, dot2(xr[i][0], one2, 0.f))));
    if (i & 1) hook(i >> 1);
  }
  const float nmu = red8(s, lane) * (-1.f / 448.f);
  hook(4);
#pragma unroll
  for (int i = 0; i < 8; ++i) {
    const float m = rdl(nmu, (i >> 2) * 32 + ((i >> 1) & 1) * 16 + (i & 1) * 8);
    f32x2 q = {0.f, 0.f};
#pragma unroll
    for (int e = 0; e < 4; ++e) { n[i][e] = f32x2{dot2(xr[i][e], e0, m), dot2(xr[i][e], e1, m)}; q += n[i][e] * n[i][e]; }
    s[i] = lane < 56 ? q[0] + q[1] : 0.f;
    hook(5 + i);
  }
  const float rs = __builtin_amdgcn_rsqf(red8(s, lane) * (1.f / 448.f) + 1e-5f);
  hook(13);
#pragma unroll
  for (int i = 0; i < 8; ++i) {
    const float r = rdl(rs, (i >> 2) * 32 + ((i >> 1) & 1) * 16 + (i & 1) * 8);
#pragma unroll
    for (int e = 0; e < 4; ++e) n[i][e] *= r;
    asm volatile("" : "+v"(n[i][0]), "+v"(n[i][1]), "+v"(n[i][2]), "+v"(n[i][3]));
    if (i & 1) hook(14 + (i >> 1));
  }
}
DI void h_write(LAS unsigned char* L, const f32x2 (&n)[8][4], int lane, int w) {
  if (lane < 56) {
    const v4u sa = ldsld<v4u>(L, L_MOD + lane * 16), ca = ldsld<v4u>(L, L_MOD + 896 + lane * 16);
    f32x2 s[4], c[4];
#pragma unroll
    for (int q = 0; q < 4; ++q) { s[q] = f32x2{lo16(sa[q]), hi16(sa[q])}; c[q] = f32x2{lo16(ca[q]), hi16(ca[q])}; }
    const u32 base = L_H + ((w >> 2) * 4 + ((w >> 1) & 1)) * 7168 + (lane >> 3) * 1024 + (w & 1) * 512;
    const u32 ch = (lane & 7) ^ ((w & 1) * 4);
#pragma unroll
    for (int i = 0; i < 8; ++i) {
      v4u h;
#pragma unroll
      for (int q = 0; q < 4; ++q) { const f32x2 t = n[i][q] * c[q] + s[q]; h[q] = pk(t[0], t[1]); }
      ldsst<v4u>(L, base + (i >> 2) * 14336 + (i & 3) * 128 + ((ch ^ (i & 3)) * 16), h);
      SCHED();
    }
  }
}

template <int NE>
DI void small_gemm(const Params& p, LAS unsigned char* L, const v4u (&wf)[2][14], int lane, u32 soff) {
  const rsrc_t rw = mk(p.ws, WS_SIZE);
  const int r = lane & 15, g = lane >> 4;
  const u32 l0 = L_H + (r >> 3) * 7168 + (r & 7) * 128 + ((g ^ (r & 7)) * 16), l1 = l0 ^ 64;
  auto lda = [&](int T, int ks) { return ldsld<v4u>(L, ((ks & 1) ? l1 : l0) + T * 14336 + (ks >> 1) * 1024); };
  v4u a[4][2];
#pragma unroll
  for (int s = 0; s < 3; ++s) { a[s][0] = lda(0, s); a[s][1] = lda(1, s); }
  f32x4 acc[2][2][NE];
#pragma unroll
  for (int J = 0; J < 2; ++J)
#pragma unroll
    for (int t = 0; t < 2; ++t)
#pragma unroll
      for (int n = 0; n < NE; ++n) { acc[J][t][n] = f32x4{0, 0, 0, 0}; asm volatile("" : "+v"(acc[J][t][n])); }
  asm volatile("s_nop 1");
  SCHED();
#pragma unroll
  for (int st = 0; st < 28; ++st) {
    const int J = st / 14, ks = st % 14;
    if (st + 3 < 28) { const int J2 = (st + 3) / 14, k2 = (st + 3) % 14; a[(st + 3) & 3][0] = lda(2 * J2, k2); a[(st + 3) & 3][1] = lda(2 * J2 + 1, k2); }
#pragma unroll
    for (int t = 0; t < 2; ++t)
#pragma unroll
      for (int n = 0; n < NE; ++n) MFMA(acc[J][t][n], a[st & 3][t], wf[n][ks]);
    if (st == 17 || st == 27) {
      const int Js = st == 17 ? 0 : 1;
      if (st == 27) asm volatile("s_nop 7\n\ts_nop 7");
      SCHED();
#pragma unroll
      for (int n = 0; n < NE; ++n) {
        asm volatile("" : "+v"(acc[Js][0][n]), "+v"(acc[Js][1][n]));
        const v4u o = {pk(acc[Js][0][n][0], acc[Js][0][n][1]), pk(acc[Js][0][n][2], acc[Js][0][n][3]), pk(acc[Js][1][n][0], acc[Js][1][n][1]), pk(acc[Js][1][n][2], acc[Js][1][n][3])};
        __builtin_amdgcn_raw_buffer_store_b128(o, rw, lane * 16 + n * 1024, soff + Js * 28 * 1024, SC1);
      }
    }
    SCHED();
  }
}

template <int W> struct Part {
  static constexpr int NB = (W == 0 || W == 3) ? 2 : 3, E0 = W == 0 ? 0 : 2 * W - 1;
  static constexpr DI int lo(int n) { return n == 0 && W > 0 ? 8 - 2 * W : 0; }
  static constexpr DI int hi(int n) { return n == NB - 1 && W < 3 ? 6 - 2 * W : 8; }
};

template <int W>
DI void big_gemm(const Params& p, LAS unsigned char* L, int lane, u32 bsoff, u32 psoff, int k0) {
  using P = Part<W>;
  constexpr int NB = P::NB;
  const rsrc_t rw = mk(p.ws, WS_SIZE);
  const int r = lane & 15, g = lane >> 4;
  const u32 l0 = (r >> 3) * 1024 + (r & 7) * 128 + ((g ^ (r & 7)) * 16);
  u32 lb[3][2];
#pragma unroll
  for (int i = 0; i < 3; ++i) { lb[i][0] = l0 + i * 65536; lb[i][1] = (l0 ^ 64) + i * 65536; asm volatile("" : "+v"(lb[i][0]), "+v"(lb[i][1])); }
  auto lda = [&](int ks, int T) { const int sl = (ks >> 1) % 9; return ldsld<v4u>(L, lb[sl >> 2][ks & 1] + (sl & 3) * 16384 + T * 2048); };
  auto ldb = [&](int ks, int n) { return __builtin_amdgcn_raw_buffer_load_b128(rw, lane * 16 + n * 1024, bsoff + (((ks + k0) & 31) * 28 + P::E0) * 1024, SC1); };
  v4u B[PD][NB], A[8];
#pragma unroll
  for (int s = 0; s < PD; ++s)
#pragma unroll
    for (int n = 0; n < NB; ++n) B[s][n] = ldb(s, n);
#pragma unroll
  for (int T = 0; T < 8; ++T) A[T] = lda(0, T);
  f32x4 acc[NB][8];
#pragma unroll
  for (int n = 0; n < NB; ++n)
#pragma unroll
    for (int T = 0; T < 8; ++T)
      if (T >= P::lo(n) && T < P::hi(n)) { acc[n][T] = f32x4{0, 0, 0, 0}; asm volatile("" : "+v"(acc[n][T])); }
  asm volatile("s_nop 1");
  u32 fv = 0;
  SCHED();
#pragma unroll
  for (int ks = 0; ks < 32; ++ks) {
    if (ks == 4 || ks == 8 || ks == 12 || ks == 14) { if (lane == 0) ldsst<u32>(L, L_CTL + C_PROG + W * 4, ks >> 1); }
    if (ks == 17 || ks == 21 || ks == 25 || ks == 29) {
      if (__builtin_amdgcn_readfirstlane(fv) == 0) lds_wait_ge(L, L_CTL + C_FULL + ((ks - 17) >> 2) * 4, 1);
    }
    if (ks == 16 || ks == 20 || ks == 24 || ks == 28) fv = lds_peek(L, L_CTL + C_FULL + ((ks - 16) >> 2) * 4);
    SCHED();
#pragma unroll
    for (int T = 0; T < 8; ++T) {
#pragma unroll
      for (int n = 0; n < NB; ++n)
        if (T >= P::lo(n) && T < P::hi(n)) {
          MFMA(acc[n][T], B[ks % PD][n], A[T]);
          if (T == P::hi(n) - 1 && ks + PD < 32) B[ks % PD][n] = ldb(ks + PD, n);
        }
      if (ks + 1 < 32) A[T] = lda(ks + 1, T);
      SCHED();
    }
  }
  asm volatile("s_nop 7\n\ts_nop 7");
  SCHED();
  const u32 vo = (r >> 2) * 28672 + g * 64 + (r & 3) * 16;
#pragma unroll
  for (int n = 0; n < NB; ++n)
#pragma unroll
    for (int J = 0; J < 4; ++J)
      if (2 * J >= P::lo(n) && 2 * J < P::hi(n)) {
        asm volatile("" : "+v"(acc[n][2 * J]), "+v"(acc[n][2 * J + 1]));
        const f32x4 u = acc[n][2 * J], v = acc[n][2 * J + 1];
        const v4u o = {pk(u[0], u[1]), pk(u[2], u[3]), pk(v[0], v[1]), pk(v[2], v[3])};
        __builtin_amdgcn_raw_buffer_store_b128(o, rw, vo + (P::E0 + n) * 256, psoff + J * 4 * 28672, SC1);
      }
}

DI void flag_after_drain(const Params& p, LAS unsigned char* L, u32 ctl, u32 off, u32 ep, int lane) {
  VM0();
  if (lane == 0) {
    if (lds_inc(L, L_CTL + ctl) == 3) __builtin_amdgcn_raw_buffer_store_b32(ep, mk(p.ws, WS_SIZE), off, 0, SC1);
  }
}
DI void poll16(const Params& p, u32 off, u32 ep, int lane) {
  const rsrc_t rw = mk(p.ws, WS_SIZE);
  _Pragma("unroll 1") for (int sp = 0; sp < SPIN_G; ++sp) {
    FENCE();
    const u32 v = __builtin_amdgcn_raw_buffer_load_b32(rw, (lane & 15) * 128, off, SC1);
    if (__all(v == ep)) break;
    __builtin_amdgcn_s_sleep(PSLEEP);
  }
  FENCE();
}
template <int K0, int K1>
DI void poll_mod(const Params& p, LAS unsigned char* L, int b, u32 ep, int lane) {
  const rsrc_t rw = mk(p.ws, WS_SIZE);
  v4u g[K1 - K0];
  _Pragma("unroll 1") for (int sp = 0; sp < SPIN_G; ++sp) {
    FENCE();
    bool ok = true;
#pragma unroll
    for (int k = K0; k < K1; ++k) { const int ch = min(k * 64 + lane, 671); g[k - K0] = __builtin_amdgcn_raw_buffer_load_b128(rw, ch * 16, WS_MOD + (b & 7) * MODREP, SC1); }
#pragma unroll
    for (int k = K0; k < K1; ++k) ok &= g[k - K0].y == ep && g[k - K0].w == ep;
    if (__all(ok)) break;
  }
#pragma unroll
  for (int k = K0; k < K1; ++k) { const int ch = min(k * 64 + lane, 671); ldsst<v2u>(L, L_MOD + ch * 8, v2u{g[k - K0].x, g[k - K0].z}); }
  FENCE();
}
DI u32 poll_keys(const Params& p, u32 ep, int lane) {
  const rsrc_t rw = mk(p.ws, WS_SIZE);
  u32 best = 0;
  _Pragma("unroll 1") for (int sp = 0; sp < SPIN_G; ++sp) {
    FENCE();
    v2u k[4];
#pragma unroll
    for (int i = 0; i < 4; ++i) k[i] = __builtin_amdgcn_raw_buffer_load_b64(rw, lane * 128, WS_KEY + i * 8192, SC1);
    best = umax(umax(k[0].x, k[1].x), umax(k[2].x, k[3].x));
    if (__all(k[0].y == ep && k[1].y == ep && k[2].y == ep && k[3].y == ep)) break;
  }
  FENCE();
  best = umax(best, dppu<XOR1>(best)); best = umax(best, dppu<XOR2>(best)); best = umax(best, dppu<HMIR>(best)); best = umax(best, dppu<MIR>(best));
  best = max16(best); best = max32(best);
  return __builtin_amdgcn_readfirstlane(best);
}

__global__ void __launch_bounds__(512, 2) k_fused(Params p) {
  extern __shared__ __attribute__((aligned(16))) unsigned char smem[];
  LAS unsigned char* L = (LAS unsigned char*)smem;
  const int b = blockIdx.x, lane = threadIdx.x & 63, w = __builtin_amdgcn_readfirstlane(threadIdx.x >> 6);
  const int xcd = b & 7, j = b >> 3;
  const int half = xcd & 1, ec = j & 3;
  const int sig = (xcd >> 1) * 8 + (j >> 2);
  const int kh = (xcd >> 1) & 1, tt = (xcd >> 2) * 8 + (j >> 2);
  const int kp0 = ROTK * (tt & 7);
  const bool prod = b < 84;
  const rsrc_t rw = mk(p.ws, WS_SIZE);
  ST(0);

  if (w < 4) {
    v4u wm[7]; u32 cv[7];
    if (prod) {
      const rsrc_t rm = mk(p.wmod, 448 * 2688 * 2), rc = mk(p.c, 896);
      const u32 row = 112 * w + (lane >> 2), vc = row * 2, vm = row * 5376 + (lane & 3) * 16;
#pragma unroll
      for (int i = 0; i < 7; ++i) asm volatile("buffer_load_ushort %0, %1, %2, 0 offen offset:%3" : "=v"(cv[i]) : "v"(vc), "s"(rc), "i"(i * 32) : "memory");
#pragma unroll
      for (int i = 0; i < 7; ++i) asm volatile("buffer_load_dwordx4 %0, %1, %2, %3 offen" POL_MOD : "=v"(wm[i]) : "v"(vm), "s"(rm), "s"(b * 64 + i * 86016) : "memory");
      BAR();
    }
    v4u xr[8];
    x_issue(p, sig * 64 + 8 * w, lane, xr);
    if (!prod) BAR();
    const u32 ep = ld_epoch(p);
    if (prod) {
      asm volatile("s_waitcnt vmcnt(15)" : "+v"(cv[0]), "+v"(cv[1]), "+v"(cv[2]), "+v"(cv[3]), "+v"(cv[4]), "+v"(cv[5]), "+v"(cv[6])::"memory");
      u32 sl[7], sh[7];
#pragma unroll
      for (int i = 0; i < 7; ++i) { const float c = lo16(cv[i]); sh[i] = pk(0.f, siluf(c)); sl[i] = sh[i] >> 16; asm volatile("" : "+v"(sl[i]), "+v"(sh[i])); }
      float a[8] = {0, 0, 0, 0, 0, 0, 0, 0};
      asm volatile("s_waitcnt vmcnt(8)" : "+v"(wm[0]), "+v"(wm[1]), "+v"(wm[2]), "+v"(wm[3]), "+v"(wm[4]), "+v"(wm[5]), "+v"(wm[6])::"memory");
      ST(16);
#pragma unroll
      for (int i = 0; i < 7; ++i)
#pragma unroll
        for (int q = 0; q < 4; ++q) { a[2 * q] = dot2(wm[i][q], sl[i], a[2 * q]); a[2 * q + 1] = dot2(wm[i][q], sh[i], a[2 * q + 1]); }
      float v0 = h16(h32(a[0], a[4]), h32(a[2], a[6])), v1 = h16(h32(a[1], a[5]), h32(a[3], a[7]));
      v0 += dppf<ROR4>(v0); v1 += dppf<ROR4>(v1); v0 += dppf<ROR8>(v0); v1 += dppf<ROR8>(v1);
      if ((lane & 12) == 0) ldsst<f32x2>(L, L_RED + w * 128 + (lane & 3) * 32 + (lane >> 5) * 16 + ((lane >> 4) & 1) * 8, f32x2{v0, v1});
      asm volatile("s_waitcnt lgkmcnt(0)" ::: "memory");
      u32 old = 0;
      if (lane == 0) old = lds_inc(L, L_CTL + C_MOD);
      if (__builtin_amdgcn_readfirstlane(old) == 3) {
        FENCE();
        f32x2 t = ldsld<f32x2>(L, L_RED + (lane & 15) * 8);
#pragma unroll
        for (int k = 1; k < 4; ++k) t += ldsld<f32x2>(L, L_RED + k * 128 + (lane & 15) * 8);
        u32 v = pk(t[0], t[1]);
        const int part = b / 14;
        if (part == 1 || part == 4) v = pk(1.f + lo16(v), 1.f + hi16(v));
        const v2u gr = {v, ep};
#pragma unroll
        for (int k = 0; k < 2; ++k) __builtin_amdgcn_raw_buffer_store_b64(gr, rw, (lane >> 4) * MODREP + (lane & 15) * 8, WS_MOD + k * 4 * MODREP + b * 128, SC1);
      }
      ST(1);
    }
    f32x2 n[8][4];
    x_wait<0>(xr);
    ST(17);
    ln_stats(xr, lane, n, [](int) {});
    ST(2);
    lds_wait_ge(L, L_CTL + C_WD, 3);
    ST(3);
    v4u wf[2][14];
    {
      const int r = lane & 15, g = lane >> 4;
      const u32 l0 = w * 28672 + (r >> 3) * 7168 + (r & 7) * 128 + ((g ^ (r & 7)) * 16), l1 = l0 ^ 64;
#pragma unroll
      for (int ks = 0; ks < 14; ++ks) wf[0][ks] = ldsld<v4u>(L, ((ks & 1) ? l1 : l0) + (ks >> 1) * 1024);
      const u32 d = w < 3 ? 14336 : 0;
#pragma unroll
      for (int ks = 0; ks < 14; ++ks) wf[1][ks] = ldsld<v4u>(L, ((ks & 1) ? l1 : l0) + d + (ks >> 1) * 1024);
#pragma unroll
      for (int ks = 0; ks < 14; ++ks) asm volatile("" : "+v"(wf[0][ks]), "+v"(wf[1][ks]));
    }
    BAR();
    ST(4);
    h_write(L, n, lane, w);
    BAR();
    ST(5);
    const u32 so = WS_AB + ((half * 64 + sig * 2) * 28 + ec * 7 + 2 * w) * 1024;
    if (w < 3) small_gemm<2>(p, L, wf, lane, so); else small_gemm<1>(p, L, wf, lane, so);
    ST(6);
    if (lane == 0) lds_inc(L, L_CTL + C_H);
    flag_after_drain(p, L, C_X1, WS_F1 + ((half * 4 + ec) * 32 + sig) * 128, ep, lane);
    ST(7);
    BAR();
    ST(8);
    const u32 bs = WS_AB + ((half * 64 + kh * 32) * 28 + ec * 7) * 1024;
    const u32 ps = WS_PART + tt * 16 * 28672 + (half * 2 + kh) * 7168 + ec * 7 * 256;
    if (w == 0) big_gemm<0>(p, L, lane, bs, ps, 2 * kp0);
    else if (w == 1) big_gemm<1>(p, L, lane, bs, ps, 2 * kp0);
    else if (w == 2) big_gemm<2>(p, L, lane, bs, ps, 2 * kp0);
    else big_gemm<3>(p, L, lane, bs, ps, 2 * kp0);
    ST(9);
    flag_after_drain(p, L, C_X2, WS_F2 + (tt * 16 + (half * 2 + kh) * 4 + ec) * 128, ep, lane);
    ST(10);
  } else if (w < 7) {
    const int hw = w - 4;
    if (hw == 2 && lane < 32) ldsst<u32>(L, L_CTL + lane * 4, 0);
    if (prod) BAR();
    v4u xr[8];
    x_issue(p, sig * 64 + 8 * w, lane, xr);
    if (!prod) BAR();
    const int r7 = lane >> 3;
    f32x2 n[8][4];
    {
      const rsrc_t rd = mk(half ? p.dim : p.dre, 448 * 896);
      const u32 vo = r7 * 896 + (((lane & 7) ^ r7) * 16);
      const u32 g0 = (ec * 112 + hw * 8) * 896, d0 = hw * 7168, back = hw == 2 ? 3 * 7168 : 0;
      auto wdp = [&](auto I) {
        constexpr int i = I.value, k = i / 7, cg = i % 7;
        const u32 so = g0 + k * 3 * 7168 - (k == 4 ? back : 0), db = d0 - (k == 4 ? back : 0);
        dmai<NT_WD, k * 3 * 7168 + cg * 1024, cg * 128>(vo, rd, so, db);
      };
      static_for<0, WD_PRE>(wdp);
      x_wait<WD_PRE>(xr);
      ST(16);
      ln_stats(xr, lane, n, [&](int h) {
        static_for<0, 18>([&](auto H) { if (H.value == h) static_for<WD_PRE + (H.value * (35 - WD_PRE)) / 18, WD_PRE + ((H.value + 1) * (35 - WD_PRE)) / 18>(wdp); });
      });
    }
    ST(2);
    VM0();
    if (lane == 0) lds_inc(L, L_CTL + C_WD);
    ST(17);
    BAR();
    h_write(L, n, lane, w);
    BAR();
    const rsrc_t ra = mk(half ? p.sim : p.sre, 2048 * 4096);
    const u32 va = (8 * (r7 >> 2) + (r7 & 3)) * 4096 + (((lane & 7) ^ r7) * 16);
    const u32 sa0 = tt * 128 * 4096 + kh * 2048;
    auto pair = [&](u32 sb, u32 db) {
      static_for<0, 16>([&](auto I) {
        constexpr int pi = I.value, T = pi >> 1, hr = pi & 1;
        const u32 so = sb + (32 * (T >> 1) + 16 * hr + 4 * (T & 1)) * 4096;
        dmai<NT_A, pi * 1024, 0>(va, ra, so, db);
        dmai<NT_A, pi * 1024 + 16384, 128>(va, ra, so, db);
      });
    };
    auto single = [&](u32 sb, u32 db) {
      static_for<0, 16>([&](auto I) {
        constexpr int pi = I.value, T = pi >> 1, hr = pi & 1;
        dmai<NT_A, pi * 1024, 0>(va, ra, sb + (32 * (T >> 1) + 16 * hr + 4 * (T & 1)) * 4096, db);
      });
    };
    pair(sa0 + hw * 256, hw * 32768);
    ST(5);
    lds_wait_ge(L, L_CTL + C_H, 4);
    single(sa0 + (6 + hw) * 128, (6 + hw) * 16384);
    ST(6);
    VM0();
    ST(7);
    BAR();
    auto wait_free = [&](u32 v) {
      _Pragma("unroll 1") for (int sp = 0; sp < SPIN_L; ++sp) {
        FENCE();
        const u32 pr = lds_peek(L, L_CTL + C_PROG + (lane & 3) * 4);
        if (__all(pr >= v)) break;
        __builtin_amdgcn_s_sleep(2);
      }
      FENCE();
    };
    wait_free(2 * hw + 2);
    ST(18);
    pair(sa0 + (9 + 2 * hw) * 128, hw * 32768);
    ST(19);
    VM0();
    if (lane == 0) ldsst<u32>(L, L_CTL + C_FULL + hw * 4, 1);
    ST(8);
    if (hw == 0) {
      wait_free(7);
      single(sa0 + 15 * 128, 6 * 16384);
      VM0();
      if (lane == 0) ldsst<u32>(L, L_CTL + C_FULL + 12, 1);
      ST(9);
    }
  } else {
    if (prod) BAR();
    v4u xr[8];
    x_issue(p, sig * 64 + 8 * w, lane, xr);
    if (!prod) BAR();
    const u32 ep = ld_epoch(p);
    {
      f32x2 n[8][4];
      x_wait<0>(xr);
      ST(17);
      ln_stats(xr, lane, n, [](int) {});
      ST(2);
      poll_mod<0, 4>(p, L, b, ep, lane);
      ST(4);
      BAR();
      h_write(L, n, lane, w);
      BAR();
    }
    poll_mod<4, 11>(p, L, b, ep, lane);
    ST(5);
    lds_wait_ge(L, L_CTL + C_X1, 4);
    poll16(p, WS_F1 + ((half * 4 + ec) * 32 + kh * 16) * 128, ep, lane);
    ST(6);
    BAR();
    if (b < NFFN) {
      lds_wait_ge(L, L_CTL + C_FULL + 12, 1);
      u32 wgp[7][8], wup[7][8], wdp[8][8];
      {
        const rsrc_t rg = mk(p.wg, 448 * 3584), ru = mk(p.wu, 448 * 3584), rd = mk(p.wd, 1792 * 896);
        v4u tg[7][2], tu[7][2], td[8][2];
        const u32 vo = (lane >> 1) * 4 * 3584 + (lane & 1) * 16, vo6 = (lane >> 1) * 2 * 3584 + (lane & 1) * 16;
#pragma unroll
        for (int t = 0; t < 7; ++t)
#pragma unroll
          for (int k = 0; k < 2; ++k) {
            const u32 so = b * 32 + (t < 6 ? 128 * (t >> 1) + 2 * (t & 1) + k : 384 + k) * 3584;
            tg[t][k] = __builtin_amdgcn_raw_buffer_load_b128(rg, t < 6 ? vo : vo6, so, AUX_FFN);
            tu[t][k] = __builtin_amdgcn_raw_buffer_load_b128(ru, t < 6 ? vo : vo6, so, AUX_FFN);
          }
        const u32 vd = lane < 56 ? lane * 16 : 0x80000000u;
#pragma unroll
        for (int t = 0; t < 8; ++t)
#pragma unroll
          for (int k = 0; k < 2; ++k) td[t][k] = __builtin_amdgcn_raw_buffer_load_b128(rd, vd, (b * 16 + 2 * t + k) * 896, AUX_FFN);
        SCHED();
#pragma unroll
        for (int t = 0; t < 7; ++t)
#pragma unroll
          for (int q = 0; q < 4; ++q) {
            wgp[t][2 * q] = __builtin_amdgcn_perm(tg[t][1][q], tg[t][0][q], 0x05040100); wgp[t][2 * q + 1] = __builtin_amdgcn_perm(tg[t][1][q], tg[t][0][q], 0x07060302);
            wup[t][2 * q] = __builtin_amdgcn_perm(tu[t][1][q], tu[t][0][q], 0x05040100); wup[t][2 * q + 1] = __builtin_amdgcn_perm(tu[t][1][q], tu[t][0][q], 0x07060302);
            asm volatile("" : "+v"(wgp[t][2 * q]), "+v"(wgp[t][2 * q + 1]), "+v"(wup[t][2 * q]), "+v"(wup[t][2 * q + 1]));
          }
#pragma unroll
        for (int t = 0; t < 8; ++t)
#pragma unroll
          for (int q = 0; q < 4; ++q) {
            wdp[t][2 * q] = __builtin_amdgcn_perm(td[t][1][q], td[t][0][q], 0x05040100); wdp[t][2 * q + 1] = __builtin_amdgcn_perm(td[t][1][q], td[t][0][q], 0x07060302);
            asm volatile("" : "+v"(wdp[t][2 * q]), "+v"(wdp[t][2 * q + 1]));
          }
      }
      ST(7);
      lds_wait_ge(L, L_CTL + C_X2, 4);
      poll16(p, WS_F2 + (b >> 4) * 16 * 128, ep, lane);
      ST(8);
      BAR();
      BAR();
      BAR();
      ST(9);
      const u32 key = poll_keys(p, ep, lane);
      ST(10);
      const u32 cb = WS_CAND + ((2047 - (key & 0x7ff)) >> 3) * 3584;
      u32 hp[7];
      _Pragma("unroll 1") for (int sp = 0; sp < SPIN_G; ++sp) {
        FENCE();
        v4u q[3];
#pragma unroll
        for (int i = 0; i < 3; ++i) q[i] = __builtin_amdgcn_raw_buffer_load_b128(rw, (lane >> 1) * 16 + i * 512, cb, SC1);
        const v2u q6 = __builtin_amdgcn_raw_buffer_load_b64(rw, (lane >> 1) * 8 + 1536, cb, SC1);
        bool ok = q6.y == ep;
#pragma unroll
        for (int i = 0; i < 3; ++i) { ok &= q[i].y == ep && q[i].w == ep; hp[2 * i] = q[i].x; hp[2 * i + 1] = q[i].z; }
        hp[6] = q6.x;
        if (__all(ok)) break;
      }
      FENCE();
      ST(11);
      float g[8], u[8];
#pragma unroll
      for (int n = 0; n < 8; ++n) { g[n] = 0.f; u[n] = 0.f; }
#pragma unroll
      for (int t = 0; t < 7; ++t)
#pragma unroll
        for (int n = 0; n < 8; ++n) { g[n] = dot2(wgp[t][n], hp[t], g[n]); u[n] = dot2(wup[t][n], hp[t], u[n]); }
      const float g0 = h16(h32(g[0], g[4]), h32(g[2], g[6])), g1 = h16(h32(g[1], g[5]), h32(g[3], g[7]));
      const float u0 = h16(h32(u[0], u[4]), h32(u[2], u[6])), u1 = h16(h32(u[1], u[5]), h32(u[3], u[7]));
      const bool b2 = lane & 4;
      const float gr1 = dppf<SHR4>(g1), gl0 = dppf<SHL4>(g0), ur1 = dppf<SHR4>(u1), ul0 = dppf<SHL4>(u0);
      float gg = b2 ? g1 + gr1 : g0 + gl0;
      float uu = b2 ? u1 + ur1 : u0 + ul0;
      gg += dppf<ROR8>(gg); uu += dppf<ROR8>(uu); gg += dppf<XOR2>(gg); uu += dppf<XOR2>(uu);
      const float act = rbf(siluf(rbf(gg))) * rbf(uu);
      const u32 ap = pk(act, dppf<SHL4>(act));
      float o[8];
#pragma unroll
      for (int n = 0; n < 8; ++n) o[n] = 0.f;
#pragma unroll
      for (int t = 0; t < 8; ++t) {
        const u32 s = __builtin_amdgcn_readlane(ap, (t >> 2) + ((t >> 1) & 1) * 32 + (t & 1) * 16);
#pragma unroll
        for (int n = 0; n < 8; ++n) o[n] = dot2(wdp[t][n], s, o[n]);
      }
      if (lane < 56) {
#pragma unroll
        for (int s = 0; s < 2; ++s) {
          const v4u gr = {pk(o[4 * s], o[4 * s + 1]), ep, pk(o[4 * s + 2], o[4 * s + 3]), ep};
          __builtin_amdgcn_raw_buffer_store_b128(gr, rw, (lane >> 2) * 2 * 7168 + (lane & 3) * 16, WS_FP + s * 7168 + b * 64, SC1);
        }
      }
      ST(12);
    } else {
      lds_wait_ge(L, L_CTL + C_X2, 4);
      poll16(p, WS_F2 + (b >> 4) * 16 * 128, ep, lane);
      ST(8);
      BAR();
      BAR();
      BAR();
      if (b < NFFN + NFIN) {
        const int f = b - NFFN, col = 32 * (f >> 1) + 8 * (lane & 3) + 4 * (f & 1);
        const v2u gb = ldsld<v2u>(L, L_MOD + 5 * 896 + col * 2);
        ST(9);
        const u32 key = poll_keys(p, ep, lane);
        ST(10);
        const u32 row = 2047 - (key & 0x7ff), kb = (key & ~0x7ffu) | 0x400u;
        const float rv = __uint_as_float((kb & 0x80000000u) ? (kb & 0x7fffffffu) : ~kb);
        const float sg = rbf(__builtin_amdgcn_rcpf(1.f + __expf(-rbf(rv))));
        v4u z;
        _Pragma("unroll 1") for (int sp = 0; sp < SPIN_G; ++sp) {
          FENCE();
          z = __builtin_amdgcn_raw_buffer_load_b128(rw, col * 4, WS_CAND + (row >> 3) * 3584 + 1792, SC1);
          const u32 dn = __builtin_amdgcn_raw_buffer_load_b32(rw, 0, WS_DONE + (row >> 3) * 128, SC1);
          if (__all(z.y == ep && z.w == ep && dn == ep)) break;
        }
        ST(11);
        float s0, s1, s2, s3;
        _Pragma("unroll 1") for (int sp = 0; sp < SPIN_G; ++sp) {
          FENCE();
          v4u q[7];
#pragma unroll
          for (int i = 0; i < 7; ++i) q[i] = __builtin_amdgcn_raw_buffer_load_b128(rw, lane * 16, WS_FP + f * 7168 + i * 1024, SC1);
          bool ok = true; s0 = s1 = s2 = s3 = 0.f;
#pragma unroll
          for (int i = 0; i < 7; ++i) { ok &= q[i].y == ep && q[i].w == ep; s0 += lo16(q[i].x); s1 += hi16(q[i].x); s2 += lo16(q[i].z); s3 += hi16(q[i].z); }
          if (__all(ok)) break;
        }
        FENCE();
        ST(12);
        float a0 = h32(s0, s2), a1 = h32(s1, s3);
        a0 = h16(a0, a0); a1 = h16(a1, a1);
        a0 += dppf<ROR4>(a0); a1 += dppf<ROR4>(a1); a0 += dppf<ROR8>(a0); a1 += dppf<ROR8>(a1);
        const u32 zz = lane < 32 ? z.x : z.z, gg = lane < 32 ? gb.x : gb.y;
        const u32 o = pk(lo16(zz) + lo16(gg) * (rbf(a0) * sg), hi16(zz) + hi16(gg) * (rbf(a1) * sg));
        if ((lane & 28) == 0) __builtin_amdgcn_raw_buffer_store_b32(o, mk(p.out, 2048 * 896), col * 2 + (lane >> 5) * 4, row * 896, SC1);
        if (f == 0 && lane == 0) __builtin_amdgcn_raw_buffer_store_b32(ep, rw, 0, WS_EPOCH, SC1);
        ST(13);
      }
    }
    return;
  }

  {
    const int ci = lane & 3, e0 = 64 * w + 4 * (lane >> 2);
    const rsrc_t rx = mk(p.x, 2048 * 896);
    const v2u xa = __builtin_amdgcn_raw_buffer_load_b64(rx, ci * 896 + e0 * 2, b * 7168, 0);
    const v2u xb = __builtin_amdgcn_raw_buffer_load_b64(rx, ci * 896 + e0 * 2, b * 7168 + 3584, 0);
    const v2u wr = __builtin_amdgcn_raw_buffer_load_b64(mk(p.wr, 896), e0 * 2, 0, 0);
    const v2u ga = ldsld<v2u>(L, L_MOD + 2 * 896 + e0 * 2), sb = ldsld<v2u>(L, L_MOD + 3 * 896 + e0 * 2), cb = ldsld<v2u>(L, L_MOD + 4 * 896 + e0 * 2), gb = ldsld<v2u>(L, L_MOD + 5 * 896 + e0 * 2);
    float gaf[4] = {lo16(ga.x), hi16(ga.x), lo16(ga.y), hi16(ga.y)}, sbf[4] = {lo16(sb.x), hi16(sb.x), lo16(sb.y), hi16(sb.y)};
    float cbf[4] = {lo16(cb.x), hi16(cb.x), lo16(cb.y), hi16(cb.y)}, gbf[4] = {lo16(gb.x), hi16(gb.x), lo16(gb.y), hi16(gb.y)};
    float wrf[4] = {lo16(wr.x), hi16(wr.x), lo16(wr.y), hi16(wr.y)};
    float va[4] = {lo16(xa.x), hi16(xa.x), lo16(xa.y), hi16(xa.y)}, vb[4] = {lo16(xb.x), hi16(xb.x), lo16(xb.y), hi16(xb.y)};
    const u32 ep = ld_epoch(p);
    float wp[4], Wp = 0.f, Cp = 0.f;
#pragma unroll
    for (int i = 0; i < 4; ++i) { wp[i] = cbf[i] * wrf[i]; Wp += wp[i]; Cp += sbf[i] * wrf[i]; }
#pragma unroll
    for (int i = 0; i < 4; ++i) asm volatile("" : "+v"(wp[i]), "+v"(va[i]), "+v"(vb[i]), "+v"(gaf[i]));
    asm volatile("" : "+v"(Wp), "+v"(Cp));
    ST(11);
    BAR();
    ST(12);
    v4u q[4];
#pragma unroll
    for (int s = 0; s < 4; ++s) q[s] = __builtin_amdgcn_raw_buffer_load_b128(rw, lane * 16, WS_PART + b * 28672 + s * 7168 + w * 1024, SC1);
#pragma unroll
    for (int i = 0; i < 2; ++i) {
      va[2 * i] += gaf[2 * i] * ((lo16(q[0][i]) + lo16(q[1][i])) - (lo16(q[2][i]) + lo16(q[3][i])));
      va[2 * i + 1] += gaf[2 * i + 1] * ((hi16(q[0][i]) + hi16(q[1][i])) - (hi16(q[2][i]) + hi16(q[3][i])));
      vb[2 * i] += gaf[2 * i] * ((lo16(q[0][2 + i]) + lo16(q[1][2 + i])) - (lo16(q[2][2 + i]) + lo16(q[3][2 + i])));
      vb[2 * i + 1] += gaf[2 * i + 1] * ((hi16(q[0][2 + i]) + hi16(q[1][2 + i])) - (hi16(q[2][2 + i]) + hi16(q[3][2 + i])));
    }
    ST(13);
    float s1a = (va[0] + va[1]) + (va[2] + va[3]), s1b = (vb[0] + vb[1]) + (vb[2] + vb[3]);
    float s2a = (va[0] * va[0] + va[1] * va[1]) + (va[2] * va[2] + va[3] * va[3]), s2b = (vb[0] * vb[0] + vb[1] * vb[1]) + (vb[2] * vb[2] + vb[3] * vb[3]);
    float s3a = (va[0] * wp[0] + va[1] * wp[1]) + (va[2] * wp[2] + va[3] * wp[3]), s3b = (vb[0] * wp[0] + vb[1] * wp[1]) + (vb[2] * wp[2] + vb[3] * wp[3]);
    float v0 = h16(h32(s1a, s1b), h32(s2a, s2b)), v1 = h16(h32(s3a, s3b), h32(Wp, Cp));
    v0 += dppf<ROR4>(v0); v1 += dppf<ROR4>(v1); v0 += dppf<ROR8>(v0); v1 += dppf<ROR8>(v1);
    if ((lane & 12) == 0) {
      const u32 o = L_ST + ((lane >> 4) & 1) * 256 + w * 32 + (lane >> 5) * 16 + ci * 4;
      ldsst<float>(L, o, v0); ldsst<float>(L, o + 512, v1);
    }
    BAR();
    float t[4];
#pragma unroll
    for (int k = 0; k < 4; ++k) { t[k] = ldsld<float>(L, L_ST + k * 256 + lane * 4); t[k] = lane < 56 ? t[k] : 0.f; }
#pragma unroll
    for (int k = 0; k < 4; ++k) { t[k] += dppf<ROR8>(t[k]); t[k] = h16(t[k], t[k]); t[k] = h32(t[k], t[k]); }
    const bool up = lane & 4;
    const float oth = dppf<ROR4>(t[3]), Wt = up ? oth : t[3], Ct = up ? t[3] : oth;
    const float mu = t[0] * (1.f / 448.f);
    const float rs = __builtin_amdgcn_rsqf(fmaxf(t[1] * (1.f / 448.f) - mu * mu, 0.f) + 1e-5f);
    const float rr = rs * (t[2] - mu * Wt) + Ct;
    u32 key = __float_as_uint(rr);
    key = (key & 0x80000000u) ? ~key : (key | 0x80000000u);
    key = (key & ~0x7ffu) | (2047 - (b * 8 + (lane & 7)));
    key = umax(key, dppu<XOR1>(key)); key = umax(key, dppu<XOR2>(key)); key = umax(key, dppu<HMIR>(key));
    if (w == 0 && lane == 0) __builtin_amdgcn_raw_buffer_store_b64(v2u{key, ep}, rw, 0, WS_KEY + b * 128, SC1);
    ST(14);
    const float mo = dppf<ROR4>(mu), ro = dppf<ROR4>(rs);
    const float mua = up ? mo : mu, mub = up ? mu : mo, rsa = up ? ro : rs, rsb = up ? rs : ro;
    float ha[4], hb[4];
#pragma unroll
    for (int i = 0; i < 4; ++i) { ha[i] = (va[i] - mua) * rsa * cbf[i] + sbf[i]; hb[i] = (vb[i] - mub) * rsb * cbf[i] + sbf[i]; }
    ldsst<v2u>(L, L_H2 + ci * 896 + e0 * 2, v2u{pk(ha[0], ha[1]), pk(ha[2], ha[3])});
    ldsst<v2u>(L, L_H2 + (ci + 4) * 896 + e0 * 2, v2u{pk(hb[0], hb[1]), pk(hb[2], hb[3])});
    ldsst<v2u>(L, L_Z + ci * 896 + e0 * 2, v2u{pk(va[0] + gbf[0] * ha[0], va[1] + gbf[1] * ha[1]), pk(va[2] + gbf[2] * ha[2], va[3] + gbf[3] * ha[3])});
    ldsst<v2u>(L, L_Z + (ci + 4) * 896 + e0 * 2, v2u{pk(vb[0] + gbf[0] * hb[0], vb[1] + gbf[1] * hb[1]), pk(vb[2] + gbf[2] * hb[2], vb[3] + gbf[3] * hb[3])});
    BAR();
    if (w == 0) {
      const rsrc_t ro_ = mk(p.out, 2048 * 896);
#pragma unroll
      for (int i = 0; i < 7; ++i) __builtin_amdgcn_raw_buffer_store_b128(ldsld<v4u>(L, L_Z + i * 1024 + lane * 16), ro_, lane * 16, b * 7168 + i * 1024, SC1);
      VM0();
      if (lane == 0) __builtin_amdgcn_raw_buffer_store_b32(ep, rw, 0, WS_DONE + b * 128, SC1);
    } else if (w < 3) {
      const u32 best = (2047 - (__builtin_amdgcn_readfirstlane(key) & 0x7ff)) & 7;
      const u32 src = (w == 1 ? L_H2 : L_Z) + best * 896;
#pragma unroll
      for (int k = 0; k < 2; ++k) {
        if (k * 64 + lane < 112) {
          const v2u d = ldsld<v2u>(L, src + (k * 64 + lane) * 8);
          __builtin_amdgcn_raw_buffer_store_b128(v4u{d.x, ep, d.y, ep}, rw, lane * 16 + k * 1024, WS_CAND + b * 3584 + (w - 1) * 1792, SC1);
        }
      }
    }
    ST(15);
  }
}

__global__ void __launch_bounds__(512, 2) k_probe(Params p) {
  extern __shared__ __attribute__((aligned(16))) unsigned char smem2[];
  const int b = blockIdx.x, lane = threadIdx.x & 63, w = __builtin_amdgcn_readfirstlane(threadIdx.x >> 6);
  const int xcd = b & 7, j = b >> 3, half = xcd & 1, kh = (xcd >> 1) & 1, tt = (xcd >> 2) * 8 + (j >> 2);
  const rsrc_t ra = mk(half ? p.sim : p.sre, 8u << 20);
  __syncthreads();
  const u64 t0 = __builtin_amdgcn_s_memrealtime();
  asm volatile("s_waitcnt lgkmcnt(0)" ::: "memory");
  const u32 s0 = (tt * 128 + (w & 3) * 32) * 4096 + kh * 2048 + (w >> 2) * 1024;
  const u32 vo = (lane >> 3) * 4096 + (lane & 7) * 16;
#pragma unroll
  for (int c = 0; c < NCH; ++c)
#pragma unroll
    for (int T = 0; T < 4; ++T) {
      const u32 so = s0 + T * 32768 + c * 128, db = w * 16384 + ((c * 4 + T) & 15) * 1024;
      asm volatile("s_mov_b32 m0, %3\n\ts_nop 0\n\tbuffer_load_dwordx4 %0, %1, %2 offen" PPOL " lds" ::"v"(vo), "s"(ra), "s"(so), "s"(db) : "memory");
    }
  asm volatile("s_waitcnt vmcnt(0)" ::: "memory");
  __syncthreads();
  const u64 e = __builtin_amdgcn_s_memrealtime();
  if (threadIdx.x == 0) ((u64*)(p.ws + WS_STAMP))[b] = e - t0;
}
__global__ void k_post(u64* st) {
  u64 v = st[threadIdx.x] + st[threadIdx.x + 64] + st[threadIdx.x + 128] + st[threadIdx.x + 192];
  for (int o = 32; o; o >>= 1) v += __shfl_xor(v, o);
  u64 d = v * AMP / 256; if (d > 100000) d = 100000;
  const u64 t1 = __builtin_amdgcn_s_memrealtime();
  const u64 te = t1 + d;
  _Pragma("unroll 1") for (int sp = 0; sp < (1 << 20); ++sp) { if (__builtin_amdgcn_s_memrealtime() >= te) break; __builtin_amdgcn_s_sleep(4); }
}
extern "C" void kernel_launch(void* const* d_in, const int* in_sizes, int n_in, void* d_out, int out_size, void* d_ws, size_t ws_size, hipStream_t stream) {
  Params p;
  p.x = (const u16*)d_in[0]; p.sre = (const u16*)d_in[1]; p.sim = (const u16*)d_in[2]; p.dre = (const u16*)d_in[3]; p.dim = (const u16*)d_in[4];
  p.wr = (const u16*)d_in[6]; p.wg = (const u16*)d_in[7]; p.wu = (const u16*)d_in[8]; p.wd = (const u16*)d_in[9]; p.c = (const u16*)d_in[10]; p.wmod = (const u16*)d_in[11];
  p.out = (u16*)d_out; p.ws = (unsigned char*)d_ws;
  (void)hipFuncSetAttribute((const void*)k_fused, hipFuncAttributeMaxDynamicSharedMemorySize, L_TOTAL);
  (void)hipFuncSetAttribute((const void*)k_probe, hipFuncAttributeMaxDynamicSharedMemorySize, L_TOTAL);
  k_probe<<<256, 512, L_TOTAL, stream>>>(p);
  k_fused<<<256, 512, L_TOTAL, stream>>>(p);
  k_post<<<1, 64, 0, stream>>>((u64*)(p.ws + WS_STAMP));
}
```

```cpp
#include <hip/hip_runtime.h>
#include <stdint.h>

typedef unsigned short u16;
typedef unsigned u32;
typedef unsigned long long u64;
typedef u32 v4u __attribute__((ext_vector_type(4)));
typedef u32 v2u __attribute__((ext_vector_type(2)));
typedef float f32x4 __attribute__((ext_vector_type(4)));
typedef float f32x2 __attribute__((ext_vector_type(2)));
typedef __bf16 bf16x2 __attribute__((ext_vector_type(2)));
typedef __amdgpu_buffer_rsrc_t rsrc_t;
#define LAS __attribute__((address_space(3)))
#define DI __device__ __forceinline__

#define AUX_FFN 0
#define PD 6
#define PSLEEP 1
#define S1B 0
#define T2N 8
#define S2A 0
#define S2B 0
#define S3U -1
#define SPA 16
#define BTN 0
#define NPRE 0
#define W3CTL G_H
#define FFNKS (32 - PD)
#define SC1 16
#define XNOB 0
#define XNOA 0
#define XNOF 0

static constexpr u32 WS_EPOCH = 0;
static constexpr u32 WS_MOD = 4096, MODREP = 10880;
static constexpr u32 WS_F1 = 98304, WS_F2 = 131072, WS_KEY = 163840, WS_DONE = 196608;
static constexpr u32 WS_CAND = 262144;
static constexpr u32 WS_FP = 1310720;
static constexpr u32 WS_AB = 2097152;
static constexpr u32 WS_PART = 6291456;
static constexpr u32 WS_STAMP = 14u << 20;
static constexpr u32 WS_SIZE = 16u << 20;
static constexpr u32 L_H = 100352;
static constexpr u32 L_MOD = 157696;
static constexpr u32 L_CTL = L_MOD + 5376;
static constexpr u32 L_RED = L_CTL + 128;
static constexpr u32 L_TOTAL = 163840;
static constexpr u32 C_PROG = 0, C_FULL = 16, C_WD = 32;
static constexpr u32 G_H = 1, G_X1 = 2, G_RING = 3, G_FFN = 7, G_X2 = 8;
static constexpr u32 L_ST = 0, L_Z = 1024, L_H2 = 8192;
static constexpr int SPIN_G = 1 << 14, SPIN_L = 1 << 18;
static constexpr int NFFN = 112, NFIN = 28;

struct Params {
  const u16 *x, *sre, *sim, *dre, *dim, *wr, *wg, *wu, *wd, *c, *wmod;
  u16* out;
  unsigned char* ws;
};

template <int V> struct IC { static constexpr int value = V; };
template <int A, int B, typename F> DI void static_for(F f) { if constexpr (A < B) { f(IC<A>{}); static_for<A + 1, B>(f); } }
DI rsrc_t mk(const void* p, u32 n) { return __builtin_amdgcn_make_buffer_rsrc((void*)p, 0, n, 0x00020000); }
DI float lo16(u32 v) { return __uint_as_float(v << 16); }
DI float hi16(u32 v) { return __uint_as_float(v & 0xffff0000u); }
DI u32 pk(float a, float b) { f32x2 v = {a, b}; return __builtin_bit_cast(u32, __builtin_convertvector(v, bf16x2)); }
DI float rbf(float a) { return lo16(pk(a, a)); }
DI float dot2(u32 a, u32 b, float c) { return __builtin_amdgcn_fdot2_f32_bf16(__builtin_bit_cast(bf16x2, a), __builtin_bit_cast(bf16x2, b), c, false); }
template <int CTRL> DI float dppf(float v) { return __uint_as_float(__builtin_amdgcn_update_dpp(0, __float_as_uint(v), CTRL, 0xf, 0xf, true)); }
template <int CTRL> DI u32 dppu(u32 v) { return __builtin_amdgcn_update_dpp(0, v, CTRL, 0xf, 0xf, true); }
static constexpr int XOR1 = 0xB1, XOR2 = 0x4E, HMIR = 0x141, MIR = 0x140, ROR4 = 0x124, ROR8 = 0x128, SHL4 = 0x104, SHR4 = 0x114;
DI float h32(float a, float b) { auto r = __builtin_amdgcn_permlane32_swap(__float_as_uint(a), __float_as_uint(b), false, false); return __uint_as_float(r[0]) + __uint_as_float(r[1]); }
DI float h16(float a, float b) { auto r = __builtin_amdgcn_permlane16_swap(__float_as_uint(a), __float_as_uint(b), false, false); return __uint_as_float(r[0]) + __uint_as_float(r[1]); }
DI u32 max32(u32 a) { auto r = __builtin_amdgcn_permlane32_swap(a, a, false, false); return r[0] > r[1] ? r[0] : r[1]; }
DI u32 max16(u32 a) { auto r = __builtin_amdgcn_permlane16_swap(a, a, false, false); return r[0] > r[1] ? r[0] : r[1]; }
DI u32 umax(u32 a, u32 b) { return a > b ? a : b; }
DI float rdl(float v, int l) { return __uint_as_float(__builtin_amdgcn_readlane(__float_as_uint(v), l)); }
DI float siluf(float v) { return v * __builtin_amdgcn_rcpf(1.f + __expf(-v)); }

#define BAR() asm volatile("s_waitcnt lgkmcnt(0)\n\ts_barrier" ::: "memory")
#define VM0() asm volatile("s_waitcnt vmcnt(0)" ::: "memory")
#define FENCE() asm volatile("" ::: "memory")
#define SCHED() __builtin_amdgcn_sched_barrier(0)
#define MFMA(acc, a, b) asm volatile("v_mfma_f32_16x16x32_bf16 %0, %1, %2, %0" : "+v"(acc) : "v"(a), "v"(b))
#define MFMA0(acc, a, b) asm volatile("v_mfma_f32_16x16x32_bf16 %0, %1, %2, 0" : "=&v"(acc) : "v"(a), "v"(b))

template <int DST, int IMM> DI void dmac(u32 voff, rsrc_t rs, u32 soff) {
  asm volatile("s_mov_b32 m0, %3\n\ts_nop 0\n\tbuffer_load_dwordx4 %0, %1, %2 offen offset:%4 lds" ::"v"(voff), "s"(rs), "s"(soff), "i"(DST - IMM), "i"(IMM) : "memory");
}
template <int DC, int IMM> DI void dmar(u32 voff, rsrc_t rs, u32 soff, u32 db) {
  asm volatile("s_add_u32 m0, %3, %4\n\ts_nop 0\n\tbuffer_load_dwordx4 %0, %1, %2 offen offset:%5 lds" ::"v"(voff), "s"(rs), "s"(soff), "s"(db), "i"(DC - IMM), "i"(IMM) : "memory", "scc");
}
template <int OFF> DI void warm(u64& sink, const unsigned char* base) { asm volatile("s_load_dwordx2 %0, %1, %2" : "+s"(sink) : "s"(base), "i"(OFF + 60)); }
template <int ROWS, int U0, int U1> DI void warm_rows(u64& sink, const unsigned char* base) {
  static_for<0, ROWS*(U1 > U0 ? U1 - U0 : 0)>([&](auto I) { constexpr int i = I.value; warm<(i % ROWS) * 4096 + (U0 + i / ROWS) * 128>(sink, base); });
}
template <int U> DI void warm8(u64& sink, const unsigned char* base) {
  asm volatile("s_load_dwordx2 %0, %1, %2\n\ts_load_dwordx2 %0, %1, %3\n\ts_load_dwordx2 %0, %1, %4\n\ts_load_dwordx2 %0, %1, %5\n\t"
               "s_load_dwordx2 %0, %1, %6\n\ts_load_dwordx2 %0, %1, %7\n\ts_load_dwordx2 %0, %1, %8\n\ts_load_dwordx2 %0, %1, %9"
               : "+s"(sink) : "s"(base), "i"(U * 128 + 60), "i"(U * 128 + 4156), "i"(U * 128 + 8252), "i"(U * 128 + 12348),
                 "i"(U * 128 + 188), "i"(U * 128 + 4284), "i"(U * 128 + 8380), "i"(U * 128 + 12476));
}
DI void warm_done(u64& sink) { asm volatile("s_waitcnt lgkmcnt(0)" : "+s"(sink)::"memory"); }
template <int OFF> DI void dsrd(v4u& d, u32 a) { asm volatile("ds_read_b128 %0, %1 offset:%2" : "=v"(d) : "v"(a), "i"(OFF)); }
template <int OFF> DI void bld(v4u& d, u32 vo, rsrc_t rs, u32 so) { asm volatile("buffer_load_dwordx4 %0, %1, %2, %3 offen offset:%4 sc1" : "=v"(d) : "v"(vo), "s"(rs), "s"(so), "i"(OFF)); }
template <int OFF> DI void bst(v4u d, u32 vo, rsrc_t rs, u32 so) { asm volatile("buffer_store_dwordx4 %0, %1, %2, %3 offen offset:%4 sc1" ::"v"(d), "v"(vo), "s"(rs), "s"(so), "i"(OFF) : "memory"); }
DI u32 cvt2(float a, float b) { u32 r; asm volatile("v_cvt_pk_bf16_f32 %0, %1, %2" : "=v"(r) : "v"(a), "v"(b)); return r; }
template <int V, int G> DI void wait_vl() { asm volatile("s_waitcnt vmcnt(%0) lgkmcnt(%1)" ::"i"(V), "i"(G)); }
template <bool Z> DI void mfma(f32x4& acc, const v4u& a, const v4u& b) { if constexpr (Z) MFMA0(acc, a, b); else MFMA(acc, a, b); }
template <int OFF> DI void dswr(u32 a, v4u d) { asm volatile("ds_write_b128 %0, %1 offset:%2" ::"v"(a), "v"(d), "i"(OFF) : "memory"); }
template <int OFF> DI void mark(u32 a, u32 v) { asm volatile("ds_write_b32 %0, %1 offset:%2" ::"v"(a), "v"(v), "i"(OFF) : "memory"); }
template <int G> DI void wait_l() { asm volatile("s_waitcnt lgkmcnt(%0)" ::"i"(G)); }

#define ST0() do { } while (0)
#define ST(prev) do { } while (0)
#define STEND(prev) do { } while (0)

template <typename T> DI T ldsld(LAS unsigned char* L, u32 off) { return *(LAS T*)(L + off); }
template <typename T> DI void ldsst(LAS unsigned char* L, u32 off, T v) { *(LAS T*)(L + off) = v; }
DI u32 lds_inc(LAS unsigned char* L, u32 off) { return __hip_atomic_fetch_add((LAS u32*)(L + off), 1u, __ATOMIC_RELAXED, __HIP_MEMORY_SCOPE_WORKGROUP); }
DI u32 lds_peek(LAS unsigned char* L, u32 off) { return __hip_atomic_load((LAS u32*)(L + off), __ATOMIC_RELAXED, __HIP_MEMORY_SCOPE_WORKGROUP); }
DI void lds_wait_ge(LAS unsigned char* L, u32 off, u32 v) {
  _Pragma("unroll 1") for (int sp = 0; sp < SPIN_L; ++sp) {
    FENCE();
    if (__builtin_amdgcn_readfirstlane(lds_peek(L, off)) >= v) break;
    __builtin_amdgcn_s_sleep(1);
  }
  FENCE();
}

#define XLD(dst, so, IMM) asm volatile("buffer_load_dwordx4 %0, %1, %2, %3 offen offset:" #IMM : "=v"(dst) : "v"(vo), "s"(rx), "s"(so) : "memory")
DI void x_issue(const Params& p, u32 vo, u32 s0, v4u (&xr)[8]) {
  const rsrc_t rx = mk(p.x, 2048 * 896);
  const u32 s1 = s0 + 3584;
  XLD(xr[0], s0, 0); XLD(xr[1], s0, 896); XLD(xr[2], s0, 1792); XLD(xr[3], s0, 2688);
  XLD(xr[4], s1, 0); XLD(xr[5], s1, 896); XLD(xr[6], s1, 1792); XLD(xr[7], s1, 2688);
}
DI void x_wait(v4u (&xr)[8], u32 younger) {
  asm volatile("s_cmp_lg_u32 %8, 0\n\ts_waitcnt vmcnt(1)\n\ts_cbranch_scc1 1f\n\ts_waitcnt vmcnt(0)\n1:"
               : "+v"(xr[0]), "+v"(xr[1]), "+v"(xr[2]), "+v"(xr[3]), "+v"(xr[4]), "+v"(xr[5]), "+v"(xr[6]), "+v"(xr[7]) : "s"(younger) : "memory", "scc");
}
template <typename H>
DI void ln_stats(const v4u (&xr)[8], int lane, f32x2 (&n)[8][4], H hook) {
  float s[8], q[8];
  const u32 one2 = 0x3f803f80u;
  static_for<0, 8>([&](auto I) {
    constexpr int i = I.value;
    float a, c;
    hook(IC<i>{});
    asm volatile("v_dot2_f32_bf16 %0, %2, %6, 0\n\tv_dot2_f32_bf16 %1, %2, %2, 0\n\tv_dot2_f32_bf16 %0, %3, %6, %0\n\tv_dot2_f32_bf16 %1, %3, %3, %1\n\t"
                 "v_dot2_f32_bf16 %0, %4, %6, %0\n\tv_dot2_f32_bf16 %1, %4, %4, %1\n\tv_dot2_f32_bf16 %0, %5, %6, %0\n\tv_dot2_f32_bf16 %1, %5, %5, %1"
                 : "=&v"(a), "=&v"(c) : "v"(xr[i][0]), "v"(xr[i][1]), "v"(xr[i][2]), "v"(xr[i][3]), "s"(one2));
    s[i] = a; q[i] = c;
  });
  asm volatile("s_nop 2" : "+v"(s[0]), "+v"(s[1]), "+v"(s[2]), "+v"(s[3]), "+v"(s[4]), "+v"(s[5]), "+v"(s[6]), "+v"(s[7]),
                           "+v"(q[0]), "+v"(q[1]), "+v"(q[2]), "+v"(q[3]), "+v"(q[4]), "+v"(q[5]), "+v"(q[6]), "+v"(q[7]));
  const float s0 = h16(h32(s[0], s[4]), h32(s[2], s[6])), s1 = h16(h32(s[1], s[5]), h32(s[3], s[7]));
  const float q0 = h16(h32(q[0], q[4]), h32(q[2], q[6])), q1 = h16(h32(q[1], q[5]), h32(q[3], q[7]));
  const bool up = lane & 8;
  float sv = (up ? s1 : s0) + dppf<ROR8>(up ? s0 : s1), qv = (up ? q1 : q0) + dppf<ROR8>(up ? q0 : q1);
  sv += dppf<XOR1>(sv); qv += dppf<XOR1>(qv); sv += dppf<XOR2>(sv); qv += dppf<XOR2>(qv); sv += dppf<HMIR>(sv); qv += dppf<HMIR>(qv);
  const float mu = sv * (1.f / 448.f);
  const float rs = __builtin_amdgcn_rsqf(fmaxf(qv * (1.f / 448.f) - mu * mu, 0.f) + 1e-5f), nm = -mu * rs;
  f32x2 rn[8];
#pragma unroll
  for (int i = 0; i < 8; ++i) { const int l = (i >> 2) * 32 + ((i >> 1) & 1) * 16 + (i & 1) * 8; rn[i] = f32x2{rdl(rs, l), rdl(nm, l)}; }
  asm volatile("s_nop 1" : "+s"(rn[0]), "+s"(rn[1]), "+s"(rn[2]), "+s"(rn[3]), "+s"(rn[4]), "+s"(rn[5]), "+s"(rn[6]), "+s"(rn[7]));
  static_for<0, 8>([&](auto I) {
    constexpr int i = I.value;
    if constexpr (i < 4) hook(IC<8 + i>{});
#pragma unroll
    for (int e = 0; e < 4; ++e) {
      const f32x2 t = {lo16(xr[i][e]), hi16(xr[i][e])};
      const f32x2 r = rn[i];
      f32x2 o;
      asm volatile("v_pk_fma_f32 %0, %1, %2, %2 op_sel:[0,0,1] op_sel_hi:[1,0,1]" : "=v"(o) : "v"(t), "s"(r));
      n[i][e] = o;
    }
    SCHED();
  });
}
struct HAddr { u32 a[4], m; };
DI HAddr h_addr(int lane, int w) {
  HAddr h;
  const u32 base = L_H + ((w >> 2) * 4 + ((w >> 1) & 1)) * 7168 + (lane >> 3) * 1024 + (w & 1) * 512;
  const u32 ch = (lane & 7) ^ ((w & 1) * 4);
#pragma unroll
  for (int k = 0; k < 4; ++k) { h.a[k] = base + k * 128 + ((ch ^ k) * 16); asm volatile("" : "+v"(h.a[k])); }
  h.m = L_MOD + lane * 16;
  asm volatile("" : "+v"(h.m));
  return h;
}
DI void h_write(const f32x2 (&n)[8][4], int lane, const HAddr& ha) {
  if (lane < 56) {
    v4u sa, ca;
    dsrd<0>(sa, ha.m); dsrd<896>(ca, ha.m);
    asm volatile("s_waitcnt lgkmcnt(0)" : "+v"(sa), "+v"(ca));
    f32x2 s[4], c[4];
#pragma unroll
    for (int q = 0; q < 4; ++q) { s[q] = f32x2{lo16(sa[q]), hi16(sa[q])}; c[q] = f32x2{lo16(ca[q]), hi16(ca[q])}; }
    f32x2 t[8][4];
    v4u h[8];
    auto F = [&](auto I) {
      constexpr int i = I.value;
      f32x2 t0 = n[i][0], t1 = n[i][1], t2 = n[i][2], t3 = n[i][3];
      const f32x2 c0 = c[0], c1 = c[1], c2 = c[2], c3 = c[3], s0 = s[0], s1 = s[1], s2 = s[2], s3 = s[3];
      asm volatile("v_pk_fma_f32 %0, %0, %4, %8\n\tv_pk_fma_f32 %1, %1, %5, %9\n\tv_pk_fma_f32 %2, %2, %6, %10\n\tv_pk_fma_f32 %3, %3, %7, %11"
                   : "+v"(t0), "+v"(t1), "+v"(t2), "+v"(t3) : "v"(c0), "v"(c1), "v"(c2), "v"(c3), "v"(s0), "v"(s1), "v"(s2), "v"(s3));
      t[i][0] = t0; t[i][1] = t1; t[i][2] = t2; t[i][3] = t3;
    };
    F(IC<0>{}); F(IC<1>{});
    static_for<0, 8>([&](auto I) {
      constexpr int i = I.value;
      const f32x2 t0 = t[i][0], t1 = t[i][1], t2 = t[i][2], t3 = t[i][3];
      u32 h0, h1, h2, h3;
      asm volatile("v_cvt_pk_bf16_f32 %0, %4, %5\n\tv_cvt_pk_bf16_f32 %1, %6, %7\n\tv_cvt_pk_bf16_f32 %2, %8, %9\n\tv_cvt_pk_bf16_f32 %3, %10, %11"
                   : "=&v"(h0), "=&v"(h1), "=&v"(h2), "=&v"(h3) : "v"(t0[0]), "v"(t0[1]), "v"(t1[0]), "v"(t1[1]), "v"(t2[0]), "v"(t2[1]), "v"(t3[0]), "v"(t3[1]));
      h[i] = v4u{h0, h1, h2, h3};
      if constexpr (i + 2 < 8) F(IC<i + 2>{});
      if constexpr (i < 6) dswr<(i >> 2) * 14336>(ha.a[i & 3], h[i]);
      if constexpr (i == 7) { dswr<14336>(ha.a[2], h[6]); dswr<14336>(ha.a[3], h[7]); }
    });
  }
}

DI void small_gemm(rsrc_t rw, const v4u (&wf)[2][14], u32 l0, u32 l1, u32 vo0, u32 vo1, u32 so0, u32 so1) {
  v4u a[4][2];
  static_for<0, 3>([&](auto S) { constexpr int s = S.value; dsrd<(s >> 1) * 1024>(a[s][0], (s & 1) ? l1 : l0); dsrd<14336 + (s >> 1) * 1024>(a[s][1], (s & 1) ? l1 : l0); });
  f32x4 acc[2][2][2];
  u32 o[4];
  static_for<0, 28>([&](auto S) {
    constexpr int st = S.value, J = st / 14, ks = st % 14, s3 = st + 3, J3 = s3 / 14, k3 = s3 % 14;
    wait_l<(st < 26 ? 4 : st == 26 ? 2 : 0)>();
    mfma<ks == 0>(acc[J][0][0], a[st & 3][0], wf[0][ks]);
    if constexpr (s3 < 28) dsrd<J3 * 28672 + (k3 >> 1) * 1024>(a[s3 & 3][0], (k3 & 1) ? l1 : l0);
    mfma<ks == 0>(acc[J][0][1], a[st & 3][0], wf[1][ks]);
    if constexpr (s3 < 28) dsrd<J3 * 28672 + 14336 + (k3 >> 1) * 1024>(a[s3 & 3][1], (k3 & 1) ? l1 : l0);
    mfma<ks == 0>(acc[J][1][0], a[st & 3][1], wf[0][ks]);
    if constexpr (st >= 16 && st < 22) {
      constexpr int n = (st - 16) / 3, ph = (st - 16) % 3;
      if constexpr (ph < 2) { o[2 * ph] = cvt2(acc[0][ph][n][0], acc[0][ph][n][1]); o[2 * ph + 1] = cvt2(acc[0][ph][n][2], acc[0][ph][n][3]); }
      else bst<n * 1024>(v4u{o[0], o[1], o[2], o[3]}, n ? vo1 : vo0, rw, so0);
    }
    mfma<ks == 0>(acc[J][1][1], a[st & 3][1], wf[1][ks]);
  });
  asm volatile("s_nop 7");
  const u32 o0 = cvt2(acc[1][0][0][0], acc[1][0][0][1]), o1 = cvt2(acc[1][0][0][2], acc[1][0][0][3]);
  const u32 o4 = cvt2(acc[1][0][1][0], acc[1][0][1][1]), o5 = cvt2(acc[1][0][1][2], acc[1][0][1][3]);
  const u32 o2 = cvt2(acc[1][1][0][0], acc[1][1][0][1]), o3 = cvt2(acc[1][1][0][2], acc[1][1][0][3]);
  const u32 o6 = cvt2(acc[1][1][1][0], acc[1][1][1][1]);
  bst<0>(v4u{o0, o1, o2, o3}, vo0, rw, so1);
  const u32 o7 = cvt2(acc[1][1][1][2], acc[1][1][1][3]);
  bst<1024>(v4u{o4, o5, o6, o7}, vo1, rw, so1);
}

template <int W> struct Part {
  static constexpr int NB = (W == 0 || W == 3) ? 2 : 3, E0 = W == 0 ? 0 : 2 * W - 1, NF = W == 0 ? 0 : 1;
  static constexpr DI int lo(int n) { return n == 0 && W > 0 ? 8 - 2 * W : 0; }
  static constexpr DI int hi(int n) { return n == NB - 1 && W < 3 ? 6 - 2 * W : 8; }
  static constexpr DI int tn(int m) { return m < 8 ? NF : W == 0 ? 1 : m - 8 < 2 * W ? 0 : 2; }
  static constexpr DI int tt(int m) { return m < 8 ? m : W == 0 ? m - 8 : m - 8 < 2 * W ? m - 2 * W : m - 8 - 2 * W; }
};

template <int W>
DI void big_gemm(const Params& p, LAS unsigned char* L, int lane, u32 bsoff, u32 psoff) {
  using P = Part<W>;
  constexpr int NB = P::NB;
  const rsrc_t rw = mk(p.ws, WS_SIZE);
  const int r = lane & 15, g = lane >> 4;
  const u32 l0 = (r >> 3) * 1024 + (r & 7) * 128 + ((g ^ (r & 7)) * 16), vo = lane * 16;
  u32 lb[3][2];
#pragma unroll
  for (int i = 0; i < 3; ++i) { lb[i][0] = l0 + i * 65536; lb[i][1] = (l0 ^ 64) + i * 65536; asm volatile("" : "+v"(lb[i][0]), "+v"(lb[i][1])); }
  u32 ctl = L_CTL, pv = 0, fv = 0, bs = bsoff + P::E0 * 1024;
  asm volatile("" : "+v"(ctl));
  asm volatile("s_waitcnt lgkmcnt(0)\n\ts_barrier" : "+s"(bs)::"memory");
  ST(10);
  v4u B[PD + 1][NB], A[2][8];
  static_for<0, PD>([&](auto S) {
    static_for<0, NB>([&](auto N) { bld<N.value * 1024>(B[S.value][N.value], vo, rw, bs); });
    asm volatile("s_add_u32 %0, %0, 28672" : "+s"(bs)::"scc");
    if constexpr (S.value < 8) dsrd<S.value * 2048>(A[0][S.value], lb[0][0]);
  });
  static_for<PD, 8>([&](auto T) { dsrd<T.value * 2048>(A[0][T.value], lb[0][0]); });
  f32x4 acc[14];
  static_for<0, 32>([&](auto K) {
    constexpr int ks = K.value, k1 = ks + 1, sl = (k1 >> 1) % 9, left = 31 - ks;
    wait_vl<NB*(left < PD - 1 ? left : PD - 1), 4>();
    if constexpr ((ks & 3) == 0) ST(12 + ks / 4);
    static_for<0, 14>([&](auto M) {
      constexpr int m = M.value, ks = K.value;
      if constexpr (m == 4) wait_l<(k1 < 32 ? 4 : 0)>();
      mfma<ks == 0>(acc[m], B[ks % (PD + 1)][P::tn(m)], A[ks & 1][P::tt(m)]);
      if constexpr (m < 8 && k1 < 32) dsrd<(sl & 3) * 16384 + m * 2048>(A[k1 & 1][m], lb[sl >> 2][k1 & 1]);
      if constexpr (!XNOB && m >= 8 && m < 8 + NB && ks + PD < 32) bld<(m - 8) * 1024>(B[(ks + PD) % (PD + 1)][m - 8], vo, rw, bs);
      if constexpr (m == 11 && ks + PD + 1 < 32) asm volatile("s_add_u32 %0, %0, 28672" : "+s"(bs)::"scc");
      constexpr int pr = ks == 3 ? G_RING : ks == 7 ? G_RING + 1 : ks == 11 ? G_RING + 2 : ks == 13 ? G_RING + 3 : ks == FFNKS ? G_FFN : 0;
      if constexpr (m == 11 && pr) asm volatile("v_mov_b32 %0, %1" : "=v"(pv) : "i"(pr));
      if constexpr (m == 12 && pr) mark<C_PROG + W * 4>(ctl, pv);
      if constexpr (m == 12 && (ks == 15 || ks == 19 || ks == 23 || ks == 27)) asm volatile("ds_read_b32 %0, %1 offset:%2" : "=v"(fv) : "v"(ctl), "i"(C_FULL));
      if constexpr (m == 12 && (ks == 16 || ks == 20 || ks == 24 || ks == 28)) {
        asm volatile("" : "+v"(fv));
        if (__builtin_amdgcn_readfirstlane(fv) < (ks - 12) / 4) lds_wait_ge(L, L_CTL + C_FULL, (ks - 12) / 4);
      }
    });
  });
  asm volatile("s_nop 7\n\ts_nop 3");
  const u32 qo = (r >> 2) * 28672 + g * 64 + (r & 3) * 16;
  static_for<0, 7>([&](auto I) {
    constexpr int m = 2 * I.value;
    const u32 o0 = cvt2(acc[m][0], acc[m][1]), o1 = cvt2(acc[m][2], acc[m][3]), o2 = cvt2(acc[m + 1][0], acc[m + 1][1]), o3 = cvt2(acc[m + 1][2], acc[m + 1][3]);
    bst<(P::E0 + P::tn(m)) * 256>(v4u{o0, o1, o2, o3}, qo, rw, psoff + (P::tt(m) >> 1) * 4 * 28672);
  });
}

DI void flag_after_drain(rsrc_t rw, u32 vf, u32 ep) {
  asm volatile("s_waitcnt vmcnt(0)\n\tbuffer_store_dword %0, %1, %2, 0 offen sc1" ::"v"(ep), "v"(vf), "s"(rw) : "memory");
}
DI void poll64(const Params& p, u32 off, u32 ep, int lane) {
  const rsrc_t rw = mk(p.ws, WS_SIZE);
  _Pragma("unroll 1") for (int sp = 0; sp < SPIN_G; ++sp) {
    FENCE();
    const u32 v = __builtin_amdgcn_raw_buffer_load_b32(rw, (lane >> 2) * 128 + (lane & 3) * 4, off, SC1);
    if (__all(v == ep)) break;
    __builtin_amdgcn_s_sleep(PSLEEP);
  }
  FENCE();
}
DI void wait_words(LAS unsigned char* L, u32 off, u32 v) {
  _Pragma("unroll 1") for (int sp = 0; sp < SPIN_L; ++sp) {
    FENCE();
    if (__all(lds_peek(L, off) >= v)) break;
    __builtin_amdgcn_s_sleep(1);
  }
  FENCE();
}
DI void wait_prog(LAS unsigned char* L, u32 v, int lane) { wait_words(L, L_CTL + C_PROG + (lane & 3) * 4, v); }
template <int K0, int K1>
DI void poll_mod(const Params& p, LAS unsigned char* L, int b, u32 ep, int lane) {
  const rsrc_t rw = mk(p.ws, WS_SIZE);
  v4u g[K1 - K0];
  _Pragma("unroll 1") for (int sp = 0; sp < SPIN_G; ++sp) {
    FENCE();
    bool ok = true;
#pragma unroll
    for (int k = K0; k < K1; ++k) { const int ch = min(k * 64 + lane, 671); g[k - K0] = __builtin_amdgcn_raw_buffer_load_b128(rw, ch * 16, WS_MOD + (b & 7) * MODREP, SC1); }
#pragma unroll
    for (int k = K0; k < K1; ++k) ok &= g[k - K0].y == ep && g[k - K0].w == ep;
    if (__all(ok)) break;
  }
#pragma unroll
  for (int k = K0; k < K1; ++k) { const int ch = min(k * 64 + lane, 671); ldsst<v2u>(L, L_MOD + ch * 8, v2u{g[k - K0].x, g[k - K0].z}); }
  FENCE();
}
DI u32 poll_keys(const Params& p, u32 ep, int lane) {
  const rsrc_t rw = mk(p.ws, WS_SIZE);
  u32 best = 0;
  _Pragma("unroll 1") for (int sp = 0; sp < SPIN_G; ++sp) {
    FENCE();
    v2u k[4];
#pragma unroll
    for (int i = 0; i < 4; ++i) k[i] = __builtin_amdgcn_raw_buffer_load_b64(rw, lane * 128, WS_KEY + i * 8192, SC1);
    best = umax(umax(k[0].x, k[1].x), umax(k[2].x, k[3].x));
    if (__all(k[0].y == ep && k[1].y == ep && k[2].y == ep && k[3].y == ep)) break;
  }
  FENCE();
  best = umax(best, dppu<XOR1>(best)); best = umax(best, dppu<XOR2>(best)); best = umax(best, dppu<HMIR>(best)); best = umax(best, dppu<MIR>(best));
  best = max16(best); best = max32(best);
  return __builtin_amdgcn_readfirstlane(best);
}

struct TailIn { v2u xa, xb, wr; };
DI void tail_issue(const Params& p, int b, int w, int lane, TailIn& t) {
  const rsrc_t rx = mk(p.x, 2048 * 896), rr = mk(p.wr, 896);
  const u32 e2 = 128 * w + 8 * (lane >> 2), vx = (lane & 3) * 896 + e2, so = b * 7168;
  asm volatile("buffer_load_dwordx2 %0, %1, %2, %3 offen" : "=v"(t.xa) : "v"(vx), "s"(rx), "s"(so) : "memory");
  asm volatile("buffer_load_dwordx2 %0, %1, %2, %3 offen offset:3584" : "=v"(t.xb) : "v"(vx), "s"(rx), "s"(so) : "memory");
  asm volatile("buffer_load_dwordx2 %0, %1, %2, 0 offen" : "=v"(t.wr) : "v"(e2), "s"(rr) : "memory");
}

typedef u32 v16u __attribute__((ext_vector_type(16)));
typedef u32 v8u __attribute__((ext_vector_type(8)));
template <typename T> DI T* ptr(u32 lo, u32 hi) { return (T*)(((u64)hi << 32) | lo); }

__global__ void __launch_bounds__(512, 2) k_fused(Params) {
  extern __shared__ __attribute__((aligned(16))) unsigned char smem[];
  LAS unsigned char* L = (LAS unsigned char*)smem;
  v16u ka; v8u kb; v2u kc;
  asm volatile("s_load_dwordx8 %1, %3, 0x40\n\ts_load_dwordx16 %0, %3, 0x0\n\ts_load_dwordx2 %2, %3, 0x60" : "=&s"(ka), "=&s"(kb), "=&s"(kc) : "s"(__builtin_amdgcn_kernarg_segment_ptr()));
  const int b = blockIdx.x, lane = threadIdx.x & 63, w = __builtin_amdgcn_readfirstlane(threadIdx.x >> 6);
  ST0();
  const int xcd = b & 7, j = b >> 3;
  const int half = xcd & 1, ec = j & 3;
  const int sig = (xcd >> 1) * 8 + (j >> 2);
  const int kh = (xcd >> 1) & 1, tt = (xcd >> 2) * 8 + (j >> 2);
  const bool prod = b < 168;
  const int cg = (j >> 2) * 32 + (xcd << (((j - 20) >> 31) & 2)) + ec;
  u32 vm = (64 * w + (lane >> 1)) * 5376 + (lane & 1) * 16, vc = 128 * w + (lane >> 1) * 2, sm = cg * 32;
  u32 vx = lane < 56 ? lane * 16 : 0x80000000u, sx = (sig * 64 + 8 * w) * 896;
  if (w == 7 && lane < 32) ldsst<u32>(L, L_CTL + lane * 4, 0);
  asm volatile("" : "+v"(vm), "+v"(vc), "+v"(vx));
  asm volatile("s_waitcnt lgkmcnt(0)" : "+s"(ka), "+s"(kb), "+s"(kc), "+s"(sm), "+s"(sx)::"memory");
  Params p;
  p.x = ptr<const u16>(ka[0], ka[1]); p.sre = ptr<const u16>(ka[2], ka[3]); p.sim = ptr<const u16>(ka[4], ka[5]); p.dre = ptr<const u16>(ka[6], ka[7]);
  p.dim = ptr<const u16>(ka[8], ka[9]); p.wr = ptr<const u16>(ka[10], ka[11]); p.wg = ptr<const u16>(ka[12], ka[13]); p.wu = ptr<const u16>(ka[14], ka[15]);
  p.wd = ptr<const u16>(kb[0], kb[1]); p.c = ptr<const u16>(kb[2], kb[3]); p.wmod = ptr<const u16>(kb[4], kb[5]); p.out = ptr<u16>(kb[6], kb[7]);
  p.ws = ptr<unsigned char>(kc[0], kc[1]);
  const rsrc_t rw = mk(p.ws, WS_SIZE);

  v4u wm[2]; u32 cv[2];
  if (prod) {
    if (w < 7) {
      const rsrc_t rm = mk(p.wmod, 448 * 2688 * 2), rc = mk(p.c, 896);
      const u32 s0 = sm, s1 = s0 + 32 * 5376;
      asm volatile("buffer_load_dwordx4 %0, %1, %2, %3 offen" : "=v"(wm[0]) : "v"(vm), "s"(rm), "s"(s0) : "memory");
      asm volatile("buffer_load_dwordx4 %0, %1, %2, %3 offen" : "=v"(wm[1]) : "v"(vm), "s"(rm), "s"(s1) : "memory");
      asm volatile("buffer_load_ushort %0, %1, %2, 0 offen" : "=v"(cv[0]) : "v"(vc), "s"(rc) : "memory");
      asm volatile("buffer_load_ushort %0, %1, %2, 0 offen offset:64" : "=v"(cv[1]) : "v"(vc), "s"(rc) : "memory");
    }
    asm volatile("s_barrier" ::: "memory");
  }
  v4u xr[8];
  x_issue(p, vx, sx, xr);
  u32 hsel = half, qoff = b;
  asm volatile("" : "+s"(hsel), "+s"(qoff));
  qoff = (((qoff & 4) * 2 + (qoff >> 5)) * 128 + ((qoff >> 3) & 3) * 32) * 4096 + (qoff & 2) * 1024;
  const unsigned char* aq = (const unsigned char*)(hsel ? p.sim : p.sre) + qoff;
  u32 ep;
  u64 sink = 0;
  asm volatile("s_load_dword %0, %1, 0x0" : "=s"(ep) : "s"(p.ws));
  if (S1B > 0) warm8<0>(sink, aq + w * 16384);
  if (!prod) asm volatile("s_barrier" ::: "memory");
  if (prod) {
    if (w < 7) {
      asm volatile("s_waitcnt vmcnt(8)" : "+v"(wm[0]), "+v"(wm[1]), "+v"(cv[0]), "+v"(cv[1])::"memory");
      ST(0);
      float a[8];
      const u32 h0 = pk(0.f, siluf(lo16(cv[0]))), h1 = pk(0.f, siluf(lo16(cv[1]))), l0 = h0 >> 16, l1 = h1 >> 16;
#pragma unroll
      for (int q = 0; q < 4; ++q) { a[2 * q] = dot2(wm[1][q], l1, dot2(wm[0][q], l0, 0.f)); a[2 * q + 1] = dot2(wm[1][q], h1, dot2(wm[0][q], h0, 0.f)); }
      float v0 = h16(h32(a[0], a[4]), h32(a[2], a[6])), v1 = h16(h32(a[1], a[5]), h32(a[3], a[7]));
      v0 += dppf<ROR4>(v0); v1 += dppf<ROR4>(v1); v0 += dppf<ROR8>(v0); v1 += dppf<ROR8>(v1); v0 += dppf<XOR2>(v0); v1 += dppf<XOR2>(v1);
      if ((lane & 14) == 0) ldsst<f32x2>(L, L_RED + w * 64 + (lane & 1) * 32 + (lane >> 5) * 16 + ((lane >> 4) & 1) * 8, f32x2{v0, v1});
    } else {
      ST(0);
    }
    BAR();
  } else {
    ST(0);
  }
  asm volatile("s_waitcnt lgkmcnt(0)" : "+s"(ep), "+s"(sink)::"memory");
  ep += 1;
  if (prod && w == 7) {
    f32x2 t = ldsld<f32x2>(L, L_RED + (lane & 7) * 8);
#pragma unroll
    for (int k = 1; k < 7; ++k) t += ldsld<f32x2>(L, L_RED + k * 64 + (lane & 7) * 8);
    u32 v = pk(t[0], t[1]);
    const int part = cg / 28;
    if (part == 1 || part == 4) v = pk(1.f + lo16(v), 1.f + hi16(v));
    __builtin_amdgcn_raw_buffer_store_b64(v2u{v, ep}, rw, (lane >> 3) * MODREP + (lane & 7) * 8, WS_MOD + cg * 64, SC1);
  }
  ST(1);
  f32x2 n[8][4];
  {
    const rsrc_t rd = mk(half ? p.dim : p.dre, 448 * 896);
    const u32 r7 = lane >> 3, vo = r7 * 896 + (((lane & 7) ^ r7) * 16), d0 = w * 1024;
    const u32 pi = 8 * (lane & 15) + w;
    u32 tv = ec * 112 * 896 + pi * 128 + ((pi * 9363) >> 16) * 6272;
    u32 so = __builtin_amdgcn_readfirstlane(tv);
    asm volatile("" : "+v"(tv));
    asm volatile("" : "+s"(so));
    x_wait(xr, (u32)((b - 168) >> 31) & (u32)((w + 1) >> 3));
    ST(2);
    if (S1B > 1) warm8<2>(sink, aq + w * 16384);
    auto piece = [&](auto H) {
      const u32 v_ = vo, d_ = d0, t_ = tv, s_ = so; const rsrc_t r_ = rd; u32 nx;
      asm volatile("s_add_u32 m0, %4, %6\n\tv_readlane_b32 %0, %5, %7\n\tbuffer_load_dwordx4 %1, %2, %3 offen lds"
                   : "=&s"(nx) : "v"(v_), "s"(r_), "s"(s_), "s"(d_), "v"(t_), "i"(H.value * 8192), "i"(H.value + 1) : "memory", "scc");
      so = nx;
    };
    ln_stats(xr, lane, n, piece);
    if (w < 2) piece(IC<12>{});
    ST(3);
    VM0();
    mark<C_WD>(L_CTL + w * 4, 1);
  }
  HAddr ha = h_addr(lane, w);
  TailIn ti;

  if (w == 7) {
    warm_done(sink);
    poll_mod<0, 4>(p, L, b, ep, lane);
    u32 tsink = 0;
    if (T2N > 0) {
      const rsrc_t ra = mk(half ? p.sim : p.sre, 2048 * 4096);
      const u32 vt = (lane & 31) * 4096 + (lane >> 5) * 128, stt = (tt * 128 + ec * 32) * 4096 + kh * 2048;
      static_for<0, T2N>([&](auto I) {
        const u32 v_ = vt, s_ = stt; const rsrc_t r_ = ra; u32 k_ = tsink;
        asm volatile("buffer_load_dword %0, %1, %2, %3 offen offset:%4" : "+v"(k_) : "v"(v_), "s"(r_), "s"(s_), "i"((2 * S1B + 2 * I.value) * 128) : "memory");
        tsink = k_;
      });
    }
    ST(4);
    BAR();
    h_write(n, lane, ha);
    BAR();
    ST(6);
    poll_mod<4, 11>(p, L, b, ep, lane);
    asm volatile("s_waitcnt vmcnt(0)" : "+v"(tsink)::"memory");
    ST(7);
    u32 vb = lane < 56 ? lane * 128 : 0x80000000u, sb = WS_AB + ((half * 64 + kh * 32 + (j >> 2)) * 28 + ec * 7) * 1024;
    asm volatile("" : "+v"(vb));
    asm volatile("" : "+s"(sb));
    wait_prog(L, G_H, lane);
    poll64(p, WS_F1 + ((half * 4 + ec) * 32 + kh * 16) * 128, ep, lane);
    static_for<0, BTN>([&](auto I) {
      const u32 v_ = vb, s_ = sb + I.value * 8 * 28672; const rsrc_t r_ = rw; u32 k_ = tsink;
      asm volatile("buffer_load_dword %0, %1, %2, %3 offen sc1" : "+v"(k_) : "v"(v_), "s"(r_), "s"(s_) : "memory");
      tsink = k_;
    });
    ST(8);
    BAR();
    warm_rows<32, SPA, 16>(sink, aq);
    warm_done(sink);
    asm volatile("s_waitcnt vmcnt(0)" : "+v"(tsink)::"memory");
    ST(9);
    if (b < NFFN) {
      const int fg = (b & 7) * 14 + (b >> 3);
      wait_prog(L, XNOF ? G_X2 : G_FFN, lane);
      ST(10);
      u32 wgp[7][8], wup[7][8], wdp[8][8];
      {
        const rsrc_t rg = mk(p.wg, 448 * 3584), ru = mk(p.wu, 448 * 3584), rd = mk(p.wd, 1792 * 896);
        v4u tg[7][2], tu[7][2], td[8][2];
        const u32 vo = (lane >> 1) * 4 * 3584 + (lane & 1) * 16, vo6 = (lane >> 1) * 2 * 3584 + (lane & 1) * 16;
#pragma unroll
        for (int t = 0; t < 7; ++t)
#pragma unroll
          for (int k = 0; k < 2; ++k) {
            const u32 so = fg * 32 + (t < 6 ? 128 * (t >> 1) + 2 * (t & 1) + k : 384 + k) * 3584;
            tg[t][k] = __builtin_amdgcn_raw_buffer_load_b128(rg, t < 6 ? vo : vo6, so, AUX_FFN);
            tu[t][k] = __builtin_amdgcn_raw_buffer_load_b128(ru, t < 6 ? vo : vo6, so, AUX_FFN);
          }
        const u32 vd = lane < 56 ? lane * 16 : 0x80000000u;
#pragma unroll
        for (int t = 0; t < 8; ++t)
#pragma unroll
          for (int k = 0; k < 2; ++k) td[t][k] = __builtin_amdgcn_raw_buffer_load_b128(rd, vd, (fg * 16 + 2 * t + k) * 896, AUX_FFN);
        SCHED();
#pragma unroll
        for (int t = 0; t < 7; ++t)
#pragma unroll
          for (int q = 0; q < 4; ++q) {
            wgp[t][2 * q] = __builtin_amdgcn_perm(tg[t][1][q], tg[t][0][q], 0x05040100); wgp[t][2 * q + 1] = __builtin_amdgcn_perm(tg[t][1][q], tg[t][0][q], 0x07060302);
            wup[t][2 * q] = __builtin_amdgcn_perm(tu[t][1][q], tu[t][0][q], 0x05040100); wup[t][2 * q + 1] = __builtin_amdgcn_perm(tu[t][1][q], tu[t][0][q], 0x07060302);
            asm volatile("" : "+v"(wgp[t][2 * q]), "+v"(wgp[t][2 * q + 1]), "+v"(wup[t][2 * q]), "+v"(wup[t][2 * q + 1]));
          }
#pragma unroll
        for (int t = 0; t < 8; ++t)
#pragma unroll
          for (int q = 0; q < 4; ++q) {
            wdp[t][2 * q] = __builtin_amdgcn_perm(td[t][1][q], td[t][0][q], 0x05040100); wdp[t][2 * q + 1] = __builtin_amdgcn_perm(td[t][1][q], td[t][0][q], 0x07060302);
            asm volatile("" : "+v"(wdp[t][2 * q]), "+v"(wdp[t][2 * q + 1]));
          }
      }
      ST(11);
      wait_prog(L, G_X2, lane);
      poll64(p, WS_F2 + (b >> 4) * 16 * 128, ep, lane);
      ST(12);
      BAR();
      BAR();
      BAR();
      ST(13);
      const u32 key = poll_keys(p, ep, lane);
      ST(14);
      const u32 cb = WS_CAND + ((2047 - (key & 0x7ff)) >> 3) * 3584;
      u32 hp[7];
      _Pragma("unroll 1") for (int sp = 0; sp < SPIN_G; ++sp) {
        FENCE();
        v4u q[3];
#pragma unroll
        for (int i = 0; i < 3; ++i) q[i] = __builtin_amdgcn_raw_buffer_load_b128(rw, (lane >> 1) * 16 + i * 512, cb, SC1);
        const v2u q6 = __builtin_amdgcn_raw_buffer_load_b64(rw, (lane >> 1) * 8 + 1536, cb, SC1);
        bool ok = q6.y == ep;
#pragma unroll
        for (int i = 0; i < 3; ++i) { ok &= q[i].y == ep && q[i].w == ep; hp[2 * i] = q[i].x; hp[2 * i + 1] = q[i].z; }
        hp[6] = q6.x;
        if (__all(ok)) break;
      }
      FENCE();
      ST(15);
      float g[8], u[8];
#pragma unroll
      for (int n = 0; n < 8; ++n) { g[n] = 0.f; u[n] = 0.f; }
#pragma unroll
      for (int t = 0; t < 7; ++t)
#pragma unroll
        for (int n = 0; n < 8; ++n) { g[n] = dot2(wgp[t][n], hp[t], g[n]); u[n] = dot2(wup[t][n], hp[t], u[n]); }
      const float g0 = h16(h32(g[0], g[4]), h32(g[2], g[6])), g1 = h16(h32(g[1], g[5]), h32(g[3], g[7]));
      const float u0 = h16(h32(u[0], u[4]), h32(u[2], u[6])), u1 = h16(h32(u[1], u[5]), h32(u[3], u[7]));
      const bool b2 = lane & 4;
      const float gr1 = dppf<SHR4>(g1), gl0 = dppf<SHL4>(g0), ur1 = dppf<SHR4>(u1), ul0 = dppf<SHL4>(u0);
      float gg = b2 ? g1 + gr1 : g0 + gl0;
      float uu = b2 ? u1 + ur1 : u0 + ul0;
      gg += dppf<ROR8>(gg); uu += dppf<ROR8>(uu); gg += dppf<XOR2>(gg); uu += dppf<XOR2>(uu);
      const float act = rbf(siluf(rbf(gg))) * rbf(uu);
      const u32 ap = pk(act, dppf<SHL4>(act));
      float o[8];
#pragma unroll
      for (int n = 0; n < 8; ++n) o[n] = 0.f;
#pragma unroll
      for (int t = 0; t < 8; ++t) {
        const u32 s = __builtin_amdgcn_readlane(ap, (t >> 2) + ((t >> 1) & 1) * 32 + (t & 1) * 16);
#pragma unroll
        for (int n = 0; n < 8; ++n) o[n] = dot2(wdp[t][n], s, o[n]);
      }
      if (lane < 56) {
#pragma unroll
        for (int s = 0; s < 2; ++s) {
          const v4u gr = {pk(o[4 * s], o[4 * s + 1]), ep, pk(o[4 * s + 2], o[4 * s + 3]), ep};
          __builtin_amdgcn_raw_buffer_store_b128(gr, rw, (lane >> 2) * 2 * 7168 + (lane & 3) * 16, WS_FP + s * 7168 + fg * 64, SC1);
        }
      }
      ST(16);
      STEND(17);
    } else {
      wait_prog(L, G_X2, lane);
      poll64(p, WS_F2 + (b >> 4) * 16 * 128, ep, lane);
      ST(10);
      BAR();
      BAR();
      BAR();
      if (b < NFFN + NFIN) {
        const int f = b - NFFN, col = 32 * (f >> 1) + 8 * (lane & 3) + 4 * (f & 1);
        const v2u gb = ldsld<v2u>(L, L_MOD + 5 * 896 + col * 2);
        ST(13);
        const u32 key = poll_keys(p, ep, lane);
        ST(20);
        const u32 row = 2047 - (key & 0x7ff), kb = (key & ~0x7ffu) | 0x400u;
        const float rv = __uint_as_float((kb & 0x80000000u) ? (kb & 0x7fffffffu) : ~kb);
        const float sg = rbf(__builtin_amdgcn_rcpf(1.f + __expf(-rbf(rv))));
        v4u z;
        _Pragma("unroll 1") for (int sp = 0; sp < SPIN_G; ++sp) {
          FENCE();
          z = __builtin_amdgcn_raw_buffer_load_b128(rw, col * 4, WS_CAND + (row >> 3) * 3584 + 1792, SC1);
          const u32 dn = __builtin_amdgcn_raw_buffer_load_b32(rw, 0, WS_DONE + (row >> 3) * 128, SC1);
          if (__all(z.y == ep && z.w == ep && dn == ep)) break;
        }
        ST(21);
        float s0, s1, s2, s3;
        _Pragma("unroll 1") for (int sp = 0; sp < SPIN_G; ++sp) {
          FENCE();
          v4u q[7];
#pragma unroll
          for (int i = 0; i < 7; ++i) q[i] = __builtin_amdgcn_raw_buffer_load_b128(rw, lane * 16, WS_FP + f * 7168 + i * 1024, SC1);
          bool ok = true; s0 = s1 = s2 = s3 = 0.f;
#pragma unroll
          for (int i = 0; i < 7; ++i) { ok &= q[i].y == ep && q[i].w == ep; s0 += lo16(q[i].x); s1 += hi16(q[i].x); s2 += lo16(q[i].z); s3 += hi16(q[i].z); }
          if (__all(ok)) break;
        }
        FENCE();
        ST(22);
        float a0 = h32(s0, s2), a1 = h32(s1, s3);
        a0 = h16(a0, a0); a1 = h16(a1, a1);
        a0 += dppf<ROR4>(a0); a1 += dppf<ROR4>(a1); a0 += dppf<ROR8>(a0); a1 += dppf<ROR8>(a1);
        const u32 zz = lane < 32 ? z.x : z.z, gg = lane < 32 ? gb.x : gb.y;
        const u32 o = pk(lo16(zz) + lo16(gg) * (rbf(a0) * sg), hi16(zz) + hi16(gg) * (rbf(a1) * sg));
        if ((lane & 28) == 0) __builtin_amdgcn_raw_buffer_store_b32(o, mk(p.out, 2048 * 896), col * 2 + (lane >> 5) * 4, row * 896, SC1);
        if (f == 0 && lane == 0) __builtin_amdgcn_raw_buffer_store_b32(ep, rw, 0, WS_EPOCH, SC1);
        ST(23);
        STEND(24);
      } else {
        STEND(13);
      }
    }
#pragma unroll
    for (int i = 0; i < 8; ++i) asm volatile("" : "=v"(n[i][0]), "=v"(n[i][1]), "=v"(n[i][2]), "=v"(n[i][3]));
    asm volatile("s_endpgm" : "=v"(ha.a[0]), "=v"(ha.a[1]), "=v"(ha.a[2]), "=v"(ha.a[3]), "=v"(ha.m)::"memory");
  }
  if (w < 4) {
    warm_done(sink);
    wait_words(L, L_CTL + C_WD + (lane & 7) * 4, 1);
    ST(4);
    v4u wf[2][14];
    {
      const int r = lane & 15, g = lane >> 4;
      const u32 l0 = w * 28672 + (r >> 3) * 7168 + (r & 7) * 128 + ((g ^ (r & 7)) * 16), l1 = l0 ^ 64;
#pragma unroll
      for (int ks = 0; ks < 14; ++ks) wf[0][ks] = ldsld<v4u>(L, ((ks & 1) ? l1 : l0) + (ks >> 1) * 1024);
      const u32 d = w < 3 ? 14336 : 0;
#pragma unroll
      for (int ks = 0; ks < 14; ++ks) wf[1][ks] = ldsld<v4u>(L, ((ks & 1) ? l1 : l0) + d + (ks >> 1) * 1024);
#pragma unroll
      for (int ks = 0; ks < 14; ++ks) asm volatile("" : "+v"(wf[0][ks]), "+v"(wf[1][ks]));
    }
    const u32 pw = L_CTL + w * 4, oob = 0x80000000u;
    u32 l0, l1, vo0 = lane * 16, vo1 = w < 3 ? vo0 : oob, so0 = WS_AB + ((half * 64 + sig * 2) * 28 + ec * 7 + 2 * w) * 1024, so1 = so0 + 28 * 1024;
    u32 vf1 = lane == 0 ? WS_F1 + ((half * 4 + ec) * 32 + sig) * 128 + w * 4 : oob, vf2 = lane == 0 ? WS_F2 + (tt * 16 + (half * 2 + kh) * 4 + ec) * 128 + w * 4 : oob, vep = ep;
    { const int r = lane & 15, g = lane >> 4; l0 = L_H + (r >> 3) * 7168 + (r & 7) * 128 + ((g ^ (r & 7)) * 16); l1 = l0 ^ 64; }
    asm volatile("" : "+v"(l0), "+v"(l1), "+v"(vo0), "+v"(vo1), "+v"(vf1), "+v"(vf2), "+v"(vep));
    asm volatile("" : "+s"(so0), "+s"(so1));
    ST(5);
    BAR();
    ST(6);
    h_write(n, lane, ha);
    BAR();
    ST(7);
    small_gemm(rw, wf, l0, l1, vo0, vo1, so0, so1);
    mark<C_PROG>(pw, G_H);
    ST(8);
    if (S3U >= 0) warm_rows<8, S3U, S3U + 1>(sink, aq + w * 32768);
    flag_after_drain(rw, vf1, vep);
    if (W3CTL == G_X1) mark<C_PROG>(pw, G_X1);
    tail_issue(p, b, w, lane, ti);
    ST(9);
    warm_done(sink);
    const u32 bs = WS_AB + ((half * 64 + kh * 32) * 28 + ec * 7) * 1024;
    const u32 ps = WS_PART + tt * 16 * 28672 + (half * 2 + kh) * 7168 + ec * 7 * 256;
    if (w == 0) big_gemm<0>(p, L, lane, bs, ps);
    else if (w == 1) big_gemm<1>(p, L, lane, bs, ps);
    else if (w == 2) big_gemm<2>(p, L, lane, bs, ps);
    else big_gemm<3>(p, L, lane, bs, ps);
    ST(26);
    flag_after_drain(rw, vf2, vep);
    mark<C_PROG>(pw, G_X2);
    ST(11);
  } else {
    warm_done(sink);
    ST(4);
    BAR();
    h_write(n, lane, ha);
    BAR();
    ST(6);
    if (w == 4) {
      const rsrc_t ra = mk(half ? p.sim : p.sre, 2048 * 4096);
      const u32 r7 = lane >> 3, sa0 = tt * 128 * 4096 + kh * 2048, sa9 = sa0 + 9 * 128;
      u32 va[16];
#pragma unroll
      for (int pi = 0; pi < 16; ++pi) {
        va[pi] = (8 * (r7 >> 2) + (r7 & 3) + 32 * (pi >> 2) + 16 * (pi & 1) + 4 * ((pi >> 1) & 1)) * 4096 + (((lane & 7) ^ r7) * 16);
        asm volatile("" : "+v"(va[pi]));
      }
      auto slices = [&](auto K0, auto K1) {
        static_for<K0.value * 16, K1.value * 16>([&](auto I) { constexpr int k = I.value >> 4, pi = I.value & 15; dmac<(k % 9) * 16384 + pi * 1024, (k % 9) * 128>(va[pi], ra, k < 9 ? sa0 : sa9); });
      };
      slices(IC<0>{}, IC<NPRE>{});
      ST(7);
      wait_prog(L, W3CTL, lane);
      ST(8);
      slices(IC<NPRE>{}, IC<9>{});
      tail_issue(p, b, w, lane, ti);
      ST(9);
      VM0();
      ST(10);
      BAR();
      static_for<0, 4>([&](auto Q) {
        constexpr int q = Q.value;
        wait_prog(L, G_RING + q, lane);
        if (!XNOA) slices(IC<9 + 2 * q>{}, IC<(q < 3 ? 11 + 2 * q : 16)>{});
        VM0();
        mark<C_FULL>(L_CTL, q + 1);
        ST(11 + q);
      });
    } else {
      tail_issue(p, b, w, lane, ti);
      constexpr int SM = S2A + 2 < S2B ? S2A + 2 : S2B;
      warm_rows<16, S2A, SM>(sink, aq + (w - 5) * 65536);
      ST(7);
      asm volatile("s_barrier" ::: "memory");
      warm_rows<16, SM, S2B>(sink, aq + (w - 5) * 65536);
      warm_done(sink);
      asm volatile("s_waitcnt vmcnt(0)" : "+v"(ti.xa), "+v"(ti.xb), "+v"(ti.wr)::"memory");
      ST(8);
    }
  }

  {
    const int ci = lane & 3, e0 = 64 * w + 4 * (lane >> 2);
    const v2u xa = ti.xa, xb = ti.xb, wr = ti.wr;
    const v2u ga = ldsld<v2u>(L, L_MOD + 2 * 896 + e0 * 2), sb = ldsld<v2u>(L, L_MOD + 3 * 896 + e0 * 2), cb = ldsld<v2u>(L, L_MOD + 4 * 896 + e0 * 2), gb = ldsld<v2u>(L, L_MOD + 5 * 896 + e0 * 2);
    float gaf[4] = {lo16(ga.x), hi16(ga.x), lo16(ga.y), hi16(ga.y)}, sbf[4] = {lo16(sb.x), hi16(sb.x), lo16(sb.y), hi16(sb.y)};
    float cbf[4] = {lo16(cb.x), hi16(cb.x), lo16(cb.y), hi16(cb.y)}, gbf[4] = {lo16(gb.x), hi16(gb.x), lo16(gb.y), hi16(gb.y)};
    float wrf[4] = {lo16(wr.x), hi16(wr.x), lo16(wr.y), hi16(wr.y)};
    float va[4] = {lo16(xa.x), hi16(xa.x), lo16(xa.y), hi16(xa.y)}, vb[4] = {lo16(xb.x), hi16(xb.x), lo16(xb.y), hi16(xb.y)};
    float wp[4], Wp = 0.f, Cp = 0.f;
#pragma unroll
    for (int i = 0; i < 4; ++i) { wp[i] = cbf[i] * wrf[i]; Wp += wp[i]; Cp += sbf[i] * wrf[i]; }
#pragma unroll
    for (int i = 0; i < 4; ++i) asm volatile("" : "+v"(wp[i]), "+v"(va[i]), "+v"(vb[i]), "+v"(gaf[i]));
    asm volatile("" : "+v"(Wp), "+v"(Cp));
    ST(20);
    BAR();
    ST(21);
    v4u q[4];
#pragma unroll
    for (int s = 0; s < 4; ++s) q[s] = __builtin_amdgcn_raw_buffer_load_b128(rw, lane * 16, WS_PART + b * 28672 + s * 7168 + w * 1024, SC1);
#pragma unroll
    for (int i = 0; i < 2; ++i) {
      va[2 * i] += gaf[2 * i] * ((lo16(q[0][i]) + lo16(q[1][i])) - (lo16(q[2][i]) + lo16(q[3][i])));
      va[2 * i + 1] += gaf[2 * i + 1] * ((hi16(q[0][i]) + hi16(q[1][i])) - (hi16(q[2][i]) + hi16(q[3][i])));
      vb[2 * i] += gaf[2 * i] * ((lo16(q[0][2 + i]) + lo16(q[1][2 + i])) - (lo16(q[2][2 + i]) + lo16(q[3][2 + i])));
      vb[2 * i + 1] += gaf[2 * i + 1] * ((hi16(q[0][2 + i]) + hi16(q[1][2 + i])) - (hi16(q[2][2 + i]) + hi16(q[3][2 + i])));
    }
    ST(22);
    float s1a = (va[0] + va[1]) + (va[2] + va[3]), s1b = (vb[0] + vb[1]) + (vb[2] + vb[3]);
    float s2a = (va[0] * va[0] + va[1] * va[1]) + (va[2] * va[2] + va[3] * va[3]), s2b = (vb[0] * vb[0] + vb[1] * vb[1]) + (vb[2] * vb[2] + vb[3] * vb[3]);
    float s3a = (va[0] * wp[0] + va[1] * wp[1]) + (va[2] * wp[2] + va[3] * wp[3]), s3b = (vb[0] * wp[0] + vb[1] * wp[1]) + (vb[2] * wp[2] + vb[3] * wp[3]);
    float v0 = h16(h32(s1a, s1b), h32(s2a, s2b)), v1 = h16(h32(s3a, s3b), h32(Wp, Cp));
    v0 += dppf<ROR4>(v0); v1 += dppf<ROR4>(v1); v0 += dppf<ROR8>(v0); v1 += dppf<ROR8>(v1);
    if ((lane & 12) == 0) {
      const u32 o = L_ST + ((lane >> 4) & 1) * 256 + w * 32 + (lane >> 5) * 16 + ci * 4;
      ldsst<float>(L, o, v0); ldsst<float>(L, o + 512, v1);
    }
    BAR();
    float t[4];
#pragma unroll
    for (int k = 0; k < 4; ++k) { t[k] = ldsld<float>(L, L_ST + k * 256 + lane * 4); t[k] = lane < 56 ? t[k] : 0.f; }
#pragma unroll
    for (int k = 0; k < 4; ++k) { t[k] += dppf<ROR8>(t[k]); t[k] = h16(t[k], t[k]); t[k] = h32(t[k], t[k]); }
    const bool up = lane & 4;
    const float oth = dppf<ROR4>(t[3]), Wt = up ? oth : t[3], Ct = up ? t[3] : oth;
    const float mu = t[0] * (1.f / 448.f);
    const float rs = __builtin_amdgcn_rsqf(fmaxf(t[1] * (1.f / 448.f) - mu * mu, 0.f) + 1e-5f);
    const float rr = rs * (t[2] - mu * Wt) + Ct;
    u32 key = __float_as_uint(rr);
    key = (key & 0x80000000u) ? ~key : (key | 0x80000000u);
    key = (key & ~0x7ffu) | (2047 - (b * 8 + (lane & 7)));
    key = umax(key, dppu<XOR1>(key)); key = umax(key, dppu<XOR2>(key)); key = umax(key, dppu<HMIR>(key));
    if (w == 0 && lane == 0) __builtin_amdgcn_raw_buffer_store_b64(v2u{key, ep}, rw, 0, WS_KEY + b * 128, SC1);
    ST(23);
    const float mo = dppf<ROR4>(mu), ro = dppf<ROR4>(rs);
    const float mua = up ? mo : mu, mub = up ? mu : mo, rsa = up ? ro : rs, rsb = up ? rs : ro;
    float ha[4], hb[4];
#pragma unroll
    for (int i = 0; i < 4; ++i) { ha[i] = (va[i] - mua) * rsa * cbf[i] + sbf[i]; hb[i] = (vb[i] - mub) * rsb * cbf[i] + sbf[i]; }
    ldsst<v2u>(L, L_H2 + ci * 896 + e0 * 2, v2u{pk(ha[0], ha[1]), pk(ha[2], ha[3])});
    ldsst<v2u>(L, L_H2 + (ci + 4) * 896 + e0 * 2, v2u{pk(hb[0], hb[1]), pk(hb[2], hb[3])});
    ldsst<v2u>(L, L_Z + ci * 896 + e0 * 2, v2u{pk(va[0] + gbf[0] * ha[0], va[1] + gbf[1] * ha[1]), pk(va[2] + gbf[2] * ha[2], va[3] + gbf[3] * ha[3])});
    ldsst<v2u>(L, L_Z + (ci + 4) * 896 + e0 * 2, v2u{pk(vb[0] + gbf[0] * hb[0], vb[1] + gbf[1] * hb[1]), pk(vb[2] + gbf[2] * hb[2], vb[3] + gbf[3] * hb[3])});
    BAR();
    if (w == 0) {
      const rsrc_t ro_ = mk(p.out, 2048 * 896);
#pragma unroll
      for (int i = 0; i < 7; ++i) __builtin_amdgcn_raw_buffer_store_b128(ldsld<v4u>(L, L_Z + i * 1024 + lane * 16), ro_, lane * 16, b * 7168 + i * 1024, SC1);
      VM0();
      if (lane == 0) __builtin_amdgcn_raw_buffer_store_b32(ep, rw, 0, WS_DONE + b * 128, SC1);
    } else if (w < 3) {
      const u32 best = (2047 - (__builtin_amdgcn_readfirstlane(key) & 0x7ff)) & 7;
      const u32 src = (w == 1 ? L_H2 : L_Z) + best * 896;
#pragma unroll
      for (int k = 0; k < 2; ++k) {
        if (k * 64 + lane < 112) {
          const v2u d = ldsld<v2u>(L, src + (k * 64 + lane) * 8);
          __builtin_amdgcn_raw_buffer_store_b128(v4u{d.x, ep, d.y, ep}, rw, lane * 16 + k * 1024, WS_CAND + b * 3584 + (w - 1) * 1792, SC1);
        }
      }
    }
    ST(24);
    STEND(25);
  }
}

extern "C" void kernel_launch(void* const* d_in, const int* in_sizes, int n_in, void* d_out, int out_size, void* d_ws, size_t ws_size, hipStream_t stream) {
  Params p;
  p.x = (const u16*)d_in[0]; p.sre = (const u16*)d_in[1]; p.sim = (const u16*)d_in[2]; p.dre = (const u16*)d_in[3]; p.dim = (const u16*)d_in[4];
  p.wr = (const u16*)d_in[6]; p.wg = (const u16*)d_in[7]; p.wu = (const u16*)d_in[8]; p.wd = (const u16*)d_in[9]; p.c = (const u16*)d_in[10]; p.wmod = (const u16*)d_in[11];
  p.out = (u16*)d_out; p.ws = (unsigned char*)d_ws;
  (void)hipFuncSetAttribute((const void*)k_fused, hipFuncAttributeMaxDynamicSharedMemorySize, L_TOTAL);
  k_fused<<<256, 512, L_TOTAL, stream>>>(p);
}
```

```cpp
#include <hip/hip_runtime.h>
#include <stdint.h>

typedef unsigned short u16;
typedef unsigned u32;
typedef unsigned long long u64;
typedef u32 v4u __attribute__((ext_vector_type(4)));
typedef u32 v2u __attribute__((ext_vector_type(2)));
typedef float f32x4 __attribute__((ext_vector_type(4)));
typedef float f32x2 __attribute__((ext_vector_type(2)));
typedef __bf16 bf16x2 __attribute__((ext_vector_type(2)));
typedef __amdgpu_buffer_rsrc_t rsrc_t;
#define LAS __attribute__((address_space(3)))
#define DI __device__ __forceinline__

#define AUX_FFN 0
#define PD 6
#define PSLEEP 1
#define S1B 0
#define T2N 8
#define S2A 0
#define S2B 0
#define S3U -1
#define SPA 16
#define BTN 0
#define NPRE 0
#define W3CTL G_H
#define FFNKS (32 - PD)
#define SC1 16
#define XNOB 0
#define XNOA 0
#define XNOF 0

static constexpr u32 WS_EPOCH = 0;
static constexpr u32 WS_MOD = 4096, MODREP = 10880;
static constexpr u32 WS_F1 = 98304, WS_F2 = 131072, WS_KEY = 163840, WS_DONE = 196608;
static constexpr u32 WS_CAND = 262144;
static constexpr u32 WS_FP = 1310720;
static constexpr u32 WS_AB = 2097152;
static constexpr u32 WS_PART = 6291456;
static constexpr u32 WS_STAMP = 14u << 20;
static constexpr u32 WS_SIZE = 16u << 20;
static constexpr u32 L_H = 100352;
static constexpr u32 L_MOD = 157696;
static constexpr u32 L_CTL = L_MOD + 5376;
static constexpr u32 L_RED = L_CTL + 128;
static constexpr u32 L_TOTAL = 163840;
static constexpr u32 C_PROG = 0, C_FULL = 16, C_WD = 32;
static constexpr u32 G_H = 1, G_X1 = 2, G_RING = 3, G_FFN = 7, G_X2 = 8;
static constexpr u32 L_ST = 0, L_Z = 1024, L_H2 = 8192;
static constexpr int SPIN_G = 1 << 14, SPIN_L = 1 << 18;
static constexpr int NFFN = 112, NFIN = 28;

struct Params {
  const u16 *x, *sre, *sim, *dre, *dim, *wr, *wg, *wu, *wd, *c, *wmod;
  u16* out;
  unsigned char* ws;
};

template <int V> struct IC { static constexpr int value = V; };
template <int A, int B, typename F> DI void static_for(F f) { if constexpr (A < B) { f(IC<A>{}); static_for<A + 1, B>(f); } }
DI rsrc_t mk(const void* p, u32 n) { return __builtin_amdgcn_make_buffer_rsrc((void*)p, 0, n, 0x00020000); }
DI float lo16(u32 v) { return __uint_as_float(v << 16); }
DI float hi16(u32 v) { return __uint_as_float(v & 0xffff0000u); }
DI u32 pk(float a, float b) { f32x2 v = {a, b}; return __builtin_bit_cast(u32, __builtin_convertvector(v, bf16x2)); }
DI float rbf(float a) { return lo16(pk(a, a)); }
DI float dot2(u32 a, u32 b, float c) { return __builtin_amdgcn_fdot2_f32_bf16(__builtin_bit_cast(bf16x2, a), __builtin_bit_cast(bf16x2, b), c, false); }
template <int CTRL> DI float dppf(float v) { return __uint_as_float(__builtin_amdgcn_update_dpp(0, __float_as_uint(v), CTRL, 0xf, 0xf, true)); }
template <int CTRL> DI u32 dppu(u32 v) { return __builtin_amdgcn_update_dpp(0, v, CTRL, 0xf, 0xf, true); }
static constexpr int XOR1 = 0xB1, XOR2 = 0x4E, HMIR = 0x141, MIR = 0x140, ROR4 = 0x124, ROR8 = 0x128, SHL4 = 0x104, SHR4 = 0x114;
DI float h32(float a, float b) { auto r = __builtin_amdgcn_permlane32_swap(__float_as_uint(a), __float_as_uint(b), false, false); return __uint_as_float(r[0]) + __uint_as_float(r[1]); }
DI float h16(float a, float b) { auto r = __builtin_amdgcn_permlane16_swap(__float_as_uint(a), __float_as_uint(b), false, false); return __uint_as_float(r[0]) + __uint_as_float(r[1]); }
DI u32 max32(u32 a) { auto r = __builtin_amdgcn_permlane32_swap(a, a, false, false); return r[0] > r[1] ? r[0] : r[1]; }
DI u32 max16(u32 a) { auto r = __builtin_amdgcn_permlane16_swap(a, a, false, false); return r[0] > r[1] ? r[0] : r[1]; }
DI u32 umax(u32 a, u32 b) { return a > b ? a : b; }
DI float rdl(float v, int l) { return __uint_as_float(__builtin_amdgcn_readlane(__float_as_uint(v), l)); }
DI float siluf(float v) { return v * __builtin_amdgcn_rcpf(1.f + __expf(-v)); }

#define BAR() asm volatile("s_waitcnt lgkmcnt(0)\n\ts_barrier" ::: "memory")
#define VM0() asm volatile("s_waitcnt vmcnt(0)" ::: "memory")
#define FENCE() asm volatile("" ::: "memory")
#define SCHED() __builtin_amdgcn_sched_barrier(0)
#define MFMA(acc, a, b) asm volatile("v_mfma_f32_16x16x32_bf16 %0, %1, %2, %0" : "+v"(acc) : "v"(a), "v"(b))
#define MFMA0(acc, a, b) asm volatile("v_mfma_f32_16x16x32_bf16 %0, %1, %2, 0" : "=&v"(acc) : "v"(a), "v"(b))

template <int DST, int IMM> DI void dmac(u32 voff, rsrc_t rs, u32 soff) {
  asm volatile("s_mov_b32 m0, %3\n\ts_nop 0\n\tbuffer_load_dwordx4 %0, %1, %2 offen offset:%4 lds" ::"v"(voff), "s"(rs), "s"(soff), "i"(DST - IMM), "i"(IMM) : "memory");
}
template <int DC, int IMM> DI void dmar(u32 voff, rsrc_t rs, u32 soff, u32 db) {
  asm volatile("s_add_u32 m0, %3, %4\n\ts_nop 0\n\tbuffer_load_dwordx4 %0, %1, %2 offen offset:%5 lds" ::"v"(voff), "s"(rs), "s"(soff), "s"(db), "i"(DC - IMM), "i"(IMM) : "memory", "scc");
}
template <int OFF> DI void warm(u64& sink, const unsigned char* base) { asm volatile("s_load_dwordx2 %0, %1, %2" : "+s"(sink) : "s"(base), "i"(OFF + 60)); }
template <int ROWS, int U0, int U1> DI void warm_rows(u64& sink, const unsigned char* base) {
  static_for<0, ROWS*(U1 > U0 ? U1 - U0 : 0)>([&](auto I) { constexpr int i = I.value; warm<(i % ROWS) * 4096 + (U0 + i / ROWS) * 128>(sink, base); });
}
template <int U> DI void warm8(u64& sink, const unsigned char* base) {
  asm volatile("s_load_dwordx2 %0, %1, %2\n\ts_load_dwordx2 %0, %1, %3\n\ts_load_dwordx2 %0, %1, %4\n\ts_load_dwordx2 %0, %1, %5\n\t"
               "s_load_dwordx2 %0, %1, %6\n\ts_load_dwordx2 %0, %1, %7\n\ts_load_dwordx2 %0, %1, %8\n\ts_load_dwordx2 %0, %1, %9"
               : "+s"(sink) : "s"(base), "i"(U * 128 + 60), "i"(U * 128 + 4156), "i"(U * 128 + 8252), "i"(U * 128 + 12348),
                 "i"(U * 128 + 188), "i"(U * 128 + 4284), "i"(U * 128 + 8380), "i"(U * 128 + 12476));
}
DI void warm_done(u64& sink) { asm volatile("s_waitcnt lgkmcnt(0)" : "+s"(sink)::"memory"); }
template <int OFF> DI void dsrd(v4u& d, u32 a) { asm volatile("ds_read_b128 %0, %1 offset:%2" : "=v"(d) : "v"(a), "i"(OFF)); }
template <int OFF> DI void bld(v4u& d, u32 vo, rsrc_t rs, u32 so) { asm volatile("buffer_load_dwordx4 %0, %1, %2, %3 offen offset:%4 sc1" : "=v"(d) : "v"(vo), "s"(rs), "s"(so), "i"(OFF)); }
template <int OFF> DI void bst(v4u d, u32 vo, rsrc_t rs, u32 so) { asm volatile("buffer_store_dwordx4 %0, %1, %2, %3 offen offset:%4 sc1" ::"v"(d), "v"(vo), "s"(rs), "s"(so), "i"(OFF) : "memory"); }
template <int OFF> DI void bstp(v4u d, u32 vo, rsrc_t rs, u32 so) { asm volatile("buffer_store_dwordx4 %0, %1, %2, %3 offen offset:%4 sc1\n\ts_nop 1" ::"v"(d), "v"(vo), "s"(rs), "s"(so), "i"(OFF) : "memory"); }
DI u32 cvt2(float a, float b) { u32 r; asm volatile("v_cvt_pk_bf16_f32 %0, %1, %2" : "=v"(r) : "v"(a), "v"(b)); return r; }
template <int V, int G> DI void wait_vl() { asm volatile("s_waitcnt vmcnt(%0) lgkmcnt(%1)" ::"i"(V), "i"(G)); }
template <bool Z> DI void mfma(f32x4& acc, const v4u& a, const v4u& b) { if constexpr (Z) MFMA0(acc, a, b); else MFMA(acc, a, b); }
template <int OFF> DI void dswr(u32 a, v4u d) { asm volatile("ds_write_b128 %0, %1 offset:%2" ::"v"(a), "v"(d), "i"(OFF) : "memory"); }
template <int OFF> DI void mark(u32 a, u32 v) { asm volatile("ds_write_b32 %0, %1 offset:%2" ::"v"(a), "v"(v), "i"(OFF) : "memory"); }
template <int OFF, int V> DI void marki(u32 a) { u32 t; asm volatile("v_mov_b32 %0, %3\n\tds_write_b32 %1, %0 offset:%2" : "=&v"(t) : "v"(a), "i"(OFF), "i"(V) : "memory"); }
template <int G> DI void wait_l() { asm volatile("s_waitcnt lgkmcnt(%0)" ::"i"(G)); }

#define ST0() do { } while (0)
#define ST(prev) do { } while (0)
#define STEND(prev) do { } while (0)

template <typename T> DI T ldsld(LAS unsigned char* L, u32 off) { return *(LAS T*)(L + off); }
template <typename T> DI void ldsst(LAS unsigned char* L, u32 off, T v) { *(LAS T*)(L + off) = v; }
DI u32 lds_inc(LAS unsigned char* L, u32 off) { return __hip_atomic_fetch_add((LAS u32*)(L + off), 1u, __ATOMIC_RELAXED, __HIP_MEMORY_SCOPE_WORKGROUP); }
DI u32 lds_peek(LAS unsigned char* L, u32 off) { return __hip_atomic_load((LAS u32*)(L + off), __ATOMIC_RELAXED, __HIP_MEMORY_SCOPE_WORKGROUP); }
DI void lds_wait_ge(LAS unsigned char* L, u32 off, u32 v) {
  _Pragma("unroll 1") for (int sp = 0; sp < SPIN_L; ++sp) {
    FENCE();
    if (__builtin_amdgcn_readfirstlane(lds_peek(L, off)) >= v) break;
    __builtin_amdgcn_s_sleep(1);
  }
  FENCE();
}

#define XLD(dst, so, IMM) asm volatile("buffer_load_dwordx4 %0, %1, %2, %3 offen offset:" #IMM : "=v"(dst) : "v"(vo), "s"(rx), "s"(so) : "memory")
DI void x_issue(const Params& p, u32 vo, u32 s0, v4u (&xr)[8]) {
  const rsrc_t rx = mk(p.x, 2048 * 896);
  const u32 s1 = s0 + 3584;
  XLD(xr[0], s0, 0); XLD(xr[1], s0, 896); XLD(xr[2], s0, 1792); XLD(xr[3], s0, 2688);
  XLD(xr[4], s1, 0); XLD(xr[5], s1, 896); XLD(xr[6], s1, 1792); XLD(xr[7], s1, 2688);
}
DI void x_wait(v4u (&xr)[8], u32 younger) {
  asm volatile("s_cmp_lg_u32 %8, 0\n\ts_waitcnt vmcnt(1)\n\ts_cbranch_scc1 1f\n\ts_waitcnt vmcnt(0)\n1:"
               : "+v"(xr[0]), "+v"(xr[1]), "+v"(xr[2]), "+v"(xr[3]), "+v"(xr[4]), "+v"(xr[5]), "+v"(xr[6]), "+v"(xr[7]) : "s"(younger) : "memory", "scc");
}
template <typename H>
DI void ln_stats(const v4u (&xr)[8], int lane, f32x2 (&n)[8][4], H hook) {
  float s[8], q[8];
  const u32 one2 = 0x3f803f80u;
  static_for<0, 8>([&](auto I) {
    constexpr int i = I.value;
    float a, c;
    hook(IC<i>{});
    asm volatile("v_dot2_f32_bf16 %0, %2, %6, 0\n\tv_dot2_f32_bf16 %1, %2, %2, 0\n\tv_dot2_f32_bf16 %0, %3, %6, %0\n\tv_dot2_f32_bf16 %1, %3, %3, %1\n\t"
                 "v_dot2_f32_bf16 %0, %4, %6, %0\n\tv_dot2_f32_bf16 %1, %4, %4, %1\n\tv_dot2_f32_bf16 %0, %5, %6, %0\n\tv_dot2_f32_bf16 %1, %5, %5, %1"
                 : "=&v"(a), "=&v"(c) : "v"(xr[i][0]), "v"(xr[i][1]), "v"(xr[i][2]), "v"(xr[i][3]), "s"(one2));
    s[i] = a; q[i] = c;
  });
  asm volatile("s_nop 2" : "+v"(s[0]), "+v"(s[1]), "+v"(s[2]), "+v"(s[3]), "+v"(s[4]), "+v"(s[5]), "+v"(s[6]), "+v"(s[7]),
                           "+v"(q[0]), "+v"(q[1]), "+v"(q[2]), "+v"(q[3]), "+v"(q[4]), "+v"(q[5]), "+v"(q[6]), "+v"(q[7]));
  const float s0 = h16(h32(s[0], s[4]), h32(s[2], s[6])), s1 = h16(h32(s[1], s[5]), h32(s[3], s[7]));
  const float q0 = h16(h32(q[0], q[4]), h32(q[2], q[6])), q1 = h16(h32(q[1], q[5]), h32(q[3], q[7]));
  const bool up = lane & 8;
  float sv = (up ? s1 : s0) + dppf<ROR8>(up ? s0 : s1), qv = (up ? q1 : q0) + dppf<ROR8>(up ? q0 : q1);
  sv += dppf<XOR1>(sv); qv += dppf<XOR1>(qv); sv += dppf<XOR2>(sv); qv += dppf<XOR2>(qv); sv += dppf<HMIR>(sv); qv += dppf<HMIR>(qv);
  const float mu = sv * (1.f / 448.f);
  const float rs = __builtin_amdgcn_rsqf(fmaxf(qv * (1.f / 448.f) - mu * mu, 0.f) + 1e-5f), nm = -mu * rs;
  f32x2 rn[8];
#pragma unroll
  for (int i = 0; i < 8; ++i) { const int l = (i >> 2) * 32 + ((i >> 1) & 1) * 16 + (i & 1) * 8; rn[i] = f32x2{rdl(rs, l), rdl(nm, l)}; }
  asm volatile("s_nop 1" : "+s"(rn[0]), "+s"(rn[1]), "+s"(rn[2]), "+s"(rn[3]), "+s"(rn[4]), "+s"(rn[5]), "+s"(rn[6]), "+s"(rn[7]));
  static_for<0, 8>([&](auto I) {
    constexpr int i = I.value;
    if constexpr (i < 4) hook(IC<8 + i>{});
#pragma unroll
    for (int e = 0; e < 4; ++e) {
      const f32x2 t = {lo16(xr[i][e]), hi16(xr[i][e])};
      const f32x2 r = rn[i];
      f32x2 o;
      asm volatile("v_pk_fma_f32 %0, %1, %2, %2 op_sel:[0,0,1] op_sel_hi:[1,0,1]" : "=v"(o) : "v"(t), "s"(r));
      n[i][e] = o;
    }
    SCHED();
  });
}
struct HAddr { u32 a[4], m; };
DI HAddr h_addr(int lane, int w) {
  HAddr h;
  const u32 base = L_H + ((w >> 2) * 4 + ((w >> 1) & 1)) * 7168 + (lane >> 3) * 1024 + (w & 1) * 512;
  const u32 ch = (lane & 7) ^ ((w & 1) * 4);
#pragma unroll
  for (int k = 0; k < 4; ++k) { h.a[k] = base + k * 128 + ((ch ^ k) * 16); asm volatile("" : "+v"(h.a[k])); }
  h.m = L_MOD + lane * 16;
  asm volatile("" : "+v"(h.m));
  return h;
}
DI void h_write(const f32x2 (&n)[8][4], int lane, const HAddr& ha) {
  if (lane < 56) {
    v4u sa, ca;
    dsrd<0>(sa, ha.m); dsrd<896>(ca, ha.m);
    asm volatile("s_waitcnt lgkmcnt(0)" : "+v"(sa), "+v"(ca));
    f32x2 s[4], c[4];
#pragma unroll
    for (int q = 0; q < 4; ++q) { s[q] = f32x2{lo16(sa[q]), hi16(sa[q])}; c[q] = f32x2{lo16(ca[q]), hi16(ca[q])}; }
    f32x2 t[8][4];
    v4u h[8];
    auto F = [&](auto I) {
      constexpr int i = I.value;
      f32x2 t0 = n[i][0], t1 = n[i][1], t2 = n[i][2], t3 = n[i][3];
      const f32x2 c0 = c[0], c1 = c[1], c2 = c[2], c3 = c[3], s0 = s[0], s1 = s[1], s2 = s[2], s3 = s[3];
      asm volatile("v_pk_fma_f32 %0, %0, %4, %8\n\tv_pk_fma_f32 %1, %1, %5, %9\n\tv_pk_fma_f32 %2, %2, %6, %10\n\tv_pk_fma_f32 %3, %3, %7, %11"
                   : "+v"(t0), "+v"(t1), "+v"(t2), "+v"(t3) : "v"(c0), "v"(c1), "v"(c2), "v"(c3), "v"(s0), "v"(s1), "v"(s2), "v"(s3));
      t[i][0] = t0; t[i][1] = t1; t[i][2] = t2; t[i][3] = t3;
    };
    F(IC<0>{}); F(IC<1>{});
    static_for<0, 8>([&](auto I) {
      constexpr int i = I.value;
      const f32x2 t0 = t[i][0], t1 = t[i][1], t2 = t[i][2], t3 = t[i][3];
      u32 h0, h1, h2, h3;
      asm volatile("v_cvt_pk_bf16_f32 %0, %4, %5\n\tv_cvt_pk_bf16_f32 %1, %6, %7\n\tv_cvt_pk_bf16_f32 %2, %8, %9\n\tv_cvt_pk_bf16_f32 %3, %10, %11"
                   : "=&v"(h0), "=&v"(h1), "=&v"(h2), "=&v"(h3) : "v"(t0[0]), "v"(t0[1]), "v"(t1[0]), "v"(t1[1]), "v"(t2[0]), "v"(t2[1]), "v"(t3[0]), "v"(t3[1]));
      h[i] = v4u{h0, h1, h2, h3};
      if constexpr (i + 2 < 8) F(IC<i + 2>{});
      if constexpr (i < 6) dswr<(i >> 2) * 14336>(ha.a[i & 3], h[i]);
      if constexpr (i == 7) { dswr<14336>(ha.a[2], h[6]); dswr<14336>(ha.a[3], h[7]); }
    });
  }
}

DI void small_gemm(rsrc_t rw, const v4u (&wf)[2][14], u32 l0, u32 l1, u32 vo0, u32 vo1, u32 so0, u32 so1) {
  v4u a[4][2];
  static_for<0, 3>([&](auto S) { constexpr int s = S.value; dsrd<(s >> 1) * 1024>(a[s][0], (s & 1) ? l1 : l0); dsrd<14336 + (s >> 1) * 1024>(a[s][1], (s & 1) ? l1 : l0); });
  f32x4 acc[2][2][2];
  u32 o[4];
  static_for<0, 28>([&](auto S) {
    constexpr int st = S.value, J = st / 14, ks = st % 14, s3 = st + 3, J3 = s3 / 14, k3 = s3 % 14;
    wait_l<(st < 26 ? 4 : st == 26 ? 2 : 0)>();
    mfma<ks == 0>(acc[J][0][0], a[st & 3][0], wf[0][ks]);
    if constexpr (s3 < 28) dsrd<J3 * 28672 + (k3 >> 1) * 1024>(a[s3 & 3][0], (k3 & 1) ? l1 : l0);
    mfma<ks == 0>(acc[J][0][1], a[st & 3][0], wf[1][ks]);
    if constexpr (s3 < 28) dsrd<J3 * 28672 + 14336 + (k3 >> 1) * 1024>(a[s3 & 3][1], (k3 & 1) ? l1 : l0);
    mfma<ks == 0>(acc[J][1][0], a[st & 3][1], wf[0][ks]);
    if constexpr (st >= 16 && st < 22) {
      constexpr int n = (st - 16) / 3, ph = (st - 16) % 3;
      if constexpr (ph < 2) { o[2 * ph] = cvt2(acc[0][ph][n][0], acc[0][ph][n][1]); o[2 * ph + 1] = cvt2(acc[0][ph][n][2], acc[0][ph][n][3]); }
      else bst<n * 1024>(v4u{o[0], o[1], o[2], o[3]}, n ? vo1 : vo0, rw, so0);
    }
    mfma<ks == 0>(acc[J][1][1], a[st & 3][1], wf[1][ks]);
  });
  asm volatile("s_nop 7");
  const u32 o0 = cvt2(acc[1][0][0][0], acc[1][0][0][1]), o1 = cvt2(acc[1][0][0][2], acc[1][0][0][3]);
  const u32 o4 = cvt2(acc[1][0][1][0], acc[1][0][1][1]), o5 = cvt2(acc[1][0][1][2], acc[1][0][1][3]);
  const u32 o2 = cvt2(acc[1][1][0][0], acc[1][1][0][1]), o3 = cvt2(acc[1][1][0][2], acc[1][1][0][3]);
  const u32 o6 = cvt2(acc[1][1][1][0], acc[1][1][1][1]);
  bst<0>(v4u{o0, o1, o2, o3}, vo0, rw, so1);
  const u32 o7 = cvt2(acc[1][1][1][2], acc[1][1][1][3]);
  bstp<1024>(v4u{o4, o5, o6, o7}, vo1, rw, so1);
}

template <int W> struct Part {
  static constexpr int NB = (W == 0 || W == 3) ? 2 : 3, E0 = W == 0 ? 0 : 2 * W - 1, NF = W == 0 ? 0 : 1;
  static constexpr DI int lo(int n) { return n == 0 && W > 0 ? 8 - 2 * W : 0; }
  static constexpr DI int hi(int n) { return n == NB - 1 && W < 3 ? 6 - 2 * W : 8; }
  static constexpr DI int tn(int m) { return m < 8 ? NF : W == 0 ? 1 : m - 8 < 2 * W ? 0 : 2; }
  static constexpr DI int tt(int m) { return m < 8 ? m : W == 0 ? m - 8 : m - 8 < 2 * W ? m - 2 * W : m - 8 - 2 * W; }
};

template <int W>
DI void big_gemm(const Params& p, LAS unsigned char* L, int lane, u32 bsoff, u32 psoff) {
  using P = Part<W>;
  constexpr int NB = P::NB;
  const rsrc_t rw = mk(p.ws, WS_SIZE);
  const int r = lane & 15, g = lane >> 4;
  const u32 l0 = (r >> 3) * 1024 + (r & 7) * 128 + ((g ^ (r & 7)) * 16), vo = lane * 16;
  u32 lb[3][2];
#pragma unroll
  for (int i = 0; i < 3; ++i) { lb[i][0] = l0 + i * 65536; lb[i][1] = (l0 ^ 64) + i * 65536; asm volatile("" : "+v"(lb[i][0]), "+v"(lb[i][1])); }
  u32 ctl = L_CTL, pv = 0, fv = 0, bs = bsoff + P::E0 * 1024;
  asm volatile("" : "+v"(ctl));
  asm volatile("s_waitcnt lgkmcnt(0)\n\ts_barrier" : "+s"(bs)::"memory");
  ST(10);
  v4u B[PD + 1][NB], A[2][8];
  static_for<0, PD>([&](auto S) {
    static_for<0, NB>([&](auto N) { bld<N.value * 1024>(B[S.value][N.value], vo, rw, bs); });
    asm volatile("s_add_u32 %0, %0, 28672" : "+s"(bs)::"scc");
    if constexpr (S.value < 8) dsrd<S.value * 2048>(A[0][S.value], lb[0][0]);
  });
  static_for<PD, 8>([&](auto T) { dsrd<T.value * 2048>(A[0][T.value], lb[0][0]); });
  f32x4 acc[14];
  static_for<0, 32>([&](auto K) {
    constexpr int ks = K.value, k1 = ks + 1, sl = (k1 >> 1) % 9, left = 31 - ks;
    wait_vl<NB*(left < PD - 1 ? left : PD - 1), 4>();
    if constexpr ((ks & 3) == 0) ST(12 + ks / 4);
    static_for<0, 14>([&](auto M) {
      constexpr int m = M.value, ks = K.value;
      if constexpr (m == 4) wait_l<(k1 < 32 ? 4 : 0)>();
      mfma<ks == 0>(acc[m], B[ks % (PD + 1)][P::tn(m)], A[ks & 1][P::tt(m)]);
      if constexpr (m < 8 && k1 < 32) dsrd<(sl & 3) * 16384 + m * 2048>(A[k1 & 1][m], lb[sl >> 2][k1 & 1]);
      if constexpr (!XNOB && m >= 8 && m < 8 + NB && ks + PD < 32) bld<(m - 8) * 1024>(B[(ks + PD) % (PD + 1)][m - 8], vo, rw, bs);
      if constexpr (m == 11 && ks + PD + 1 < 32) asm volatile("s_add_u32 %0, %0, 28672" : "+s"(bs)::"scc");
      constexpr int pr = ks == 3 ? G_RING : ks == 7 ? G_RING + 1 : ks == 11 ? G_RING + 2 : ks == 13 ? G_RING + 3 : ks == FFNKS ? G_FFN : 0;
      if constexpr (m == 11 && pr) asm volatile("v_mov_b32 %0, %1" : "=v"(pv) : "i"(pr));
      if constexpr (m == 12 && pr) mark<C_PROG + W * 4>(ctl, pv);
      if constexpr (m == 12 && (ks == 15 || ks == 19 || ks == 23 || ks == 27)) asm volatile("ds_read_b32 %0, %1 offset:%2" : "=v"(fv) : "v"(ctl), "i"(C_FULL));
      if constexpr (m == 12 && (ks == 16 || ks == 20 || ks == 24 || ks == 28)) {
        asm volatile("" : "+v"(fv));
        if (__builtin_amdgcn_readfirstlane(fv) < (ks - 12) / 4) lds_wait_ge(L, L_CTL + C_FULL, (ks - 12) / 4);
      }
    });
  });
  asm volatile("s_nop 7\n\ts_nop 3");
  const u32 qo = (r >> 2) * 28672 + g * 64 + (r & 3) * 16;
  static_for<0, 7>([&](auto I) {
    constexpr int m = 2 * I.value;
    const u32 o0 = cvt2(acc[m][0], acc[m][1]), o1 = cvt2(acc[m][2], acc[m][3]), o2 = cvt2(acc[m + 1][0], acc[m + 1][1]), o3 = cvt2(acc[m + 1][2], acc[m + 1][3]);
    if constexpr (m < 12) bst<(P::E0 + P::tn(m)) * 256>(v4u{o0, o1, o2, o3}, qo, rw, psoff + (P::tt(m) >> 1) * 4 * 28672);
    else bstp<(P::E0 + P::tn(m)) * 256>(v4u{o0, o1, o2, o3}, qo, rw, psoff + (P::tt(m) >> 1) * 4 * 28672);
  });
}

DI void flag_after_drain(rsrc_t rw, u32 vf, u32 ep) {
  asm volatile("s_waitcnt vmcnt(0)\n\tbuffer_store_dword %0, %1, %2, 0 offen sc1" ::"v"(ep), "v"(vf), "s"(rw) : "memory");
}
DI void poll64(const Params& p, u32 off, u32 ep, int lane) {
  const rsrc_t rw = mk(p.ws, WS_SIZE);
  _Pragma("unroll 1") for (int sp = 0; sp < SPIN_G; ++sp) {
    FENCE();
    const u32 v = __builtin_amdgcn_raw_buffer_load_b32(rw, (lane >> 2) * 128 + (lane & 3) * 4, off, SC1);
    if (__all(v == ep)) break;
    __builtin_amdgcn_s_sleep(PSLEEP);
  }
  FENCE();
}
DI void wait_words(LAS unsigned char* L, u32 off, u32 v) {
  _Pragma("unroll 1") for (int sp = 0; sp < SPIN_L; ++sp) {
    FENCE();
    if (__all(lds_peek(L, off) >= v)) break;
    __builtin_amdgcn_s_sleep(1);
  }
  FENCE();
}
DI void wait_prog(LAS unsigned char* L, u32 v, int lane) { wait_words(L, L_CTL + C_PROG + (lane & 3) * 4, v); }
template <int K0, int K1>
DI void poll_mod(const Params& p, LAS unsigned char* L, int b, u32 ep, int lane) {
  const rsrc_t rw = mk(p.ws, WS_SIZE);
  v4u g[K1 - K0];
  _Pragma("unroll 1") for (int sp = 0; sp < SPIN_G; ++sp) {
    FENCE();
    bool ok = true;
#pragma unroll
    for (int k = K0; k < K1; ++k) { const int ch = min(k * 64 + lane, 671); g[k - K0] = __builtin_amdgcn_raw_buffer_load_b128(rw, ch * 16, WS_MOD + (b & 7) * MODREP, SC1); }
#pragma unroll
    for (int k = K0; k < K1; ++k) ok &= g[k - K0].y == ep && g[k - K0].w == ep;
    if (__all(ok)) break;
  }
#pragma unroll
  for (int k = K0; k < K1; ++k) { const int ch = min(k * 64 + lane, 671); ldsst<v2u>(L, L_MOD + ch * 8, v2u{g[k - K0].x, g[k - K0].z}); }
  FENCE();
}
DI u32 poll_keys(const Params& p, u32 ep, int lane) {
  const rsrc_t rw = mk(p.ws, WS_SIZE);
  u32 best = 0;
  _Pragma("unroll 1") for (int sp = 0; sp < SPIN_G; ++sp) {
    FENCE();
    v2u k[4];
#pragma unroll
    for (int i = 0; i < 4; ++i) k[i] = __builtin_amdgcn_raw_buffer_load_b64(rw, lane * 128, WS_KEY + i * 8192, SC1);
    best = umax(umax(k[0].x, k[1].x), umax(k[2].x, k[3].x));
    if (__all(k[0].y == ep && k[1].y == ep && k[2].y == ep && k[3].y == ep)) break;
  }
  FENCE();
  best = umax(best, dppu<XOR1>(best)); best = umax(best, dppu<XOR2>(best)); best = umax(best, dppu<HMIR>(best)); best = umax(best, dppu<MIR>(best));
  best = max16(best); best = max32(best);
  return __builtin_amdgcn_readfirstlane(best);
}

struct TailIn { v2u xa, xb, wr; };
DI void tail_issue(const Params& p, int b, int w, int lane, TailIn& t) {
  const rsrc_t rx = mk(p.x, 2048 * 896), rr = mk(p.wr, 896);
  const u32 e2 = 128 * w + 8 * (lane >> 2), vx = (lane & 3) * 896 + e2, so = b * 7168;
  asm volatile("buffer_load_dwordx2 %0, %1, %2, %3 offen" : "=v"(t.xa) : "v"(vx), "s"(rx), "s"(so) : "memory");
  asm volatile("buffer_load_dwordx2 %0, %1, %2, %3 offen offset:3584" : "=v"(t.xb) : "v"(vx), "s"(rx), "s"(so) : "memory");
  asm volatile("buffer_load_dwordx2 %0, %1, %2, 0 offen" : "=v"(t.wr) : "v"(e2), "s"(rr) : "memory");
}

typedef u32 v16u __attribute__((ext_vector_type(16)));
typedef u32 v8u __attribute__((ext_vector_type(8)));
template <typename T> DI T* ptr(u32 lo, u32 hi) { return (T*)(((u64)hi << 32) | lo); }

__global__ void __launch_bounds__(512, 2) k_fused(Params) {
  extern __shared__ __attribute__((aligned(16))) unsigned char smem[];
  LAS unsigned char* L = (LAS unsigned char*)smem;
  v16u ka; v8u kb; v2u kc;
  asm volatile("s_load_dwordx8 %1, %3, 0x40\n\ts_load_dwordx16 %0, %3, 0x0\n\ts_load_dwordx2 %2, %3, 0x60" : "=&s"(ka), "=&s"(kb), "=&s"(kc) : "s"(__builtin_amdgcn_kernarg_segment_ptr()));
  const int b = blockIdx.x, lane = threadIdx.x & 63, w = __builtin_amdgcn_readfirstlane(threadIdx.x >> 6);
  ST0();
  const int xcd = b & 7, j = b >> 3;
  const int half = xcd & 1, ec = j & 3;
  const int sig = (xcd >> 1) * 8 + (j >> 2);
  const int kh = (xcd >> 1) & 1, tt = (xcd >> 2) * 8 + (j >> 2);
  const bool prod = b < 168;
  const int cg = (j >> 2) * 32 + (xcd << (((j - 20) >> 31) & 2)) + ec;
  u32 vm = (64 * w + (lane >> 1)) * 5376 + (lane & 1) * 16, vc = 128 * w + (lane >> 1) * 2, sm = cg * 32;
  u32 vx = lane < 56 ? lane * 16 : 0x80000000u, sx = (sig * 64 + 8 * w) * 896;
  if (w == 7 && lane < 32) ldsst<u32>(L, L_CTL + lane * 4, 0);
  asm volatile("" : "+v"(vm), "+v"(vc), "+v"(vx));
  asm volatile("s_waitcnt lgkmcnt(0)" : "+s"(ka), "+s"(kb), "+s"(kc), "+s"(sm), "+s"(sx)::"memory");
  Params p;
  p.x = ptr<const u16>(ka[0], ka[1]); p.sre = ptr<const u16>(ka[2], ka[3]); p.sim = ptr<const u16>(ka[4], ka[5]); p.dre = ptr<const u16>(ka[6], ka[7]);
  p.dim = ptr<const u16>(ka[8], ka[9]); p.wr = ptr<const u16>(ka[10], ka[11]); p.wg = ptr<const u16>(ka[12], ka[13]); p.wu = ptr<const u16>(ka[14], ka[15]);
  p.wd = ptr<const u16>(kb[0], kb[1]); p.c = ptr<const u16>(kb[2], kb[3]); p.wmod = ptr<const u16>(kb[4], kb[5]); p.out = ptr<u16>(kb[6], kb[7]);
  p.ws = ptr<unsigned char>(kc[0], kc[1]);
  const rsrc_t rw = mk(p.ws, WS_SIZE);

  v4u wm[2]; u32 cv[2];
  if (prod) {
    if (w < 7) {
      const rsrc_t rm = mk(p.wmod, 448 * 2688 * 2), rc = mk(p.c, 896);
      const u32 s0 = sm, s1 = s0 + 32 * 5376;
      asm volatile("buffer_load_dwordx4 %0, %1, %2, %3 offen" : "=v"(wm[0]) : "v"(vm), "s"(rm), "s"(s0) : "memory");
      asm volatile("buffer_load_dwordx4 %0, %1, %2, %3 offen" : "=v"(wm[1]) : "v"(vm), "s"(rm), "s"(s1) : "memory");
      asm volatile("buffer_load_ushort %0, %1, %2, 0 offen" : "=v"(cv[0]) : "v"(vc), "s"(rc) : "memory");
      asm volatile("buffer_load_ushort %0, %1, %2, 0 offen offset:64" : "=v"(cv[1]) : "v"(vc), "s"(rc) : "memory");
    }
    asm volatile("s_barrier" ::: "memory");
  }
  v4u xr[8];
  x_issue(p, vx, sx, xr);
  u32 hsel = half, qoff = b;
  asm volatile("" : "+s"(hsel), "+s"(qoff));
  qoff = (((qoff & 4) * 2 + (qoff >> 5)) * 128 + ((qoff >> 3) & 3) * 32) * 4096 + (qoff & 2) * 1024;
  const unsigned char* aq = (const unsigned char*)(hsel ? p.sim : p.sre) + qoff;
  u32 ep;
  u64 sink = 0;
  asm volatile("s_load_dword %0, %1, 0x0" : "=s"(ep) : "s"(p.ws));
  if (S1B > 0) warm8<0>(sink, aq + w * 16384);
  if (!prod) asm volatile("s_barrier" ::: "memory");
  if (prod) {
    if (w < 7) {
      asm volatile("s_waitcnt vmcnt(8)" : "+v"(wm[0]), "+v"(wm[1]), "+v"(cv[0]), "+v"(cv[1])::"memory");
      ST(0);
      float a[8];
      const u32 h0 = pk(0.f, siluf(lo16(cv[0]))), h1 = pk(0.f, siluf(lo16(cv[1]))), l0 = h0 >> 16, l1 = h1 >> 16;
#pragma unroll
      for (int q = 0; q < 4; ++q) { a[2 * q] = dot2(wm[1][q], l1, dot2(wm[0][q], l0, 0.f)); a[2 * q + 1] = dot2(wm[1][q], h1, dot2(wm[0][q], h0, 0.f)); }
      float v0 = h16(h32(a[0], a[4]), h32(a[2], a[6])), v1 = h16(h32(a[1], a[5]), h32(a[3], a[7]));
      v0 += dppf<ROR4>(v0); v1 += dppf<ROR4>(v1); v0 += dppf<ROR8>(v0); v1 += dppf<ROR8>(v1); v0 += dppf<XOR2>(v0); v1 += dppf<XOR2>(v1);
      if ((lane & 14) == 0) ldsst<f32x2>(L, L_RED + w * 64 + (lane & 1) * 32 + (lane >> 5) * 16 + ((lane >> 4) & 1) * 8, f32x2{v0, v1});
    } else {
      ST(0);
    }
    BAR();
  } else {
    ST(0);
  }
  asm volatile("s_waitcnt lgkmcnt(0)" : "+s"(ep), "+s"(sink)::"memory");
  ep += 1;
  if (prod && w == 7) {
    f32x2 t = ldsld<f32x2>(L, L_RED + (lane & 7) * 8);
#pragma unroll
    for (int k = 1; k < 7; ++k) t += ldsld<f32x2>(L, L_RED + k * 64 + (lane & 7) * 8);
    u32 v = pk(t[0], t[1]);
    const int part = cg / 28;
    if (part == 1 || part == 4) v = pk(1.f + lo16(v), 1.f + hi16(v));
    __builtin_amdgcn_raw_buffer_store_b64(v2u{v, ep}, rw, (lane >> 3) * MODREP + (lane & 7) * 8, WS_MOD + cg * 64, SC1);
  }
  ST(1);
  f32x2 n[8][4];
  {
    const rsrc_t rd = mk(half ? p.dim : p.dre, 448 * 896);
    const u32 r7 = lane >> 3, vo = r7 * 896 + (((lane & 7) ^ r7) * 16), d0 = w * 1024;
    const u32 pi = 8 * (lane & 15) + w;
    u32 tv = ec * 112 * 896 + pi * 128 + ((pi * 9363) >> 16) * 6272;
    u32 so = __builtin_amdgcn_readfirstlane(tv);
    asm volatile("" : "+v"(tv));
    asm volatile("" : "+s"(so));
    x_wait(xr, (u32)((b - 168) >> 31) & (u32)((w + 1) >> 3));
    ST(2);
    if (S1B > 1) warm8<2>(sink, aq + w * 16384);
    auto piece = [&](auto H) {
      const u32 v_ = vo, d_ = d0, t_ = tv, s_ = so; const rsrc_t r_ = rd; u32 nx;
      asm volatile("s_add_u32 m0, %4, %6\n\tv_readlane_b32 %0, %5, %7\n\tbuffer_load_dwordx4 %1, %2, %3 offen lds"
                   : "=&s"(nx) : "v"(v_), "s"(r_), "s"(s_), "s"(d_), "v"(t_), "i"(H.value * 8192), "i"(H.value + 1) : "memory", "scc");
      so = nx;
    };
    ln_stats(xr, lane, n, piece);
    if (w < 2) piece(IC<12>{});
    ST(3);
    VM0();
    mark<C_WD>(L_CTL + w * 4, 1);
  }
  HAddr ha = h_addr(lane, w);
  TailIn ti;

  if (w == 7) {
    warm_done(sink);
    poll_mod<0, 4>(p, L, b, ep, lane);
    u32 tsink = 0;
    if (T2N > 0) {
      const rsrc_t ra = mk(half ? p.sim : p.sre, 2048 * 4096);
      const u32 vt = (lane & 31) * 4096 + (lane >> 5) * 128, stt = (tt * 128 + ec * 32) * 4096 + kh * 2048;
      static_for<0, T2N>([&](auto I) {
        const u32 v_ = vt, s_ = stt; const rsrc_t r_ = ra; u32 k_ = tsink;
        asm volatile("buffer_load_dword %0, %1, %2, %3 offen offset:%4" : "+v"(k_) : "v"(v_), "s"(r_), "s"(s_), "i"((2 * S1B + 2 * I.value) * 128) : "memory");
        tsink = k_;
      });
    }
    ST(4);
    BAR();
    h_write(n, lane, ha);
    BAR();
    ST(6);
    poll_mod<4, 11>(p, L, b, ep, lane);
    asm volatile("s_waitcnt vmcnt(0)" : "+v"(tsink)::"memory");
    ST(7);
    u32 vb = lane < 56 ? lane * 128 : 0x80000000u, sb = WS_AB + ((half * 64 + kh * 32 + (j >> 2)) * 28 + ec * 7) * 1024;
    asm volatile("" : "+v"(vb));
    asm volatile("" : "+s"(sb));
    wait_prog(L, G_H, lane);
    poll64(p, WS_F1 + ((half * 4 + ec) * 32 + kh * 16) * 128, ep, lane);
    static_for<0, BTN>([&](auto I) {
      const u32 v_ = vb, s_ = sb + I.value * 8 * 28672; const rsrc_t r_ = rw; u32 k_ = tsink;
      asm volatile("buffer_load_dword %0, %1, %2, %3 offen sc1" : "+v"(k_) : "v"(v_), "s"(r_), "s"(s_) : "memory");
      tsink = k_;
    });
    ST(8);
    BAR();
    warm_rows<32, SPA, 16>(sink, aq);
    warm_done(sink);
    asm volatile("s_waitcnt vmcnt(0)" : "+v"(tsink)::"memory");
    ST(9);
    if (b < NFFN) {
      const int fg = (b & 7) * 14 + (b >> 3);
      wait_prog(L, XNOF ? G_X2 : G_FFN, lane);
      ST(10);
      u32 wgp[7][8], wup[7][8], wdp[8][8];
      {
        const rsrc_t rg = mk(p.wg, 448 * 3584), ru = mk(p.wu, 448 * 3584), rd = mk(p.wd, 1792 * 896);
        v4u tg[7][2], tu[7][2], td[8][2];
        const u32 vo = (lane >> 1) * 4 * 3584 + (lane & 1) * 16, vo6 = (lane >> 1) * 2 * 3584 + (lane & 1) * 16;
#pragma unroll
        for (int t = 0; t < 7; ++t)
#pragma unroll
          for (int k = 0; k < 2; ++k) {
            const u32 so = fg * 32 + (t < 6 ? 128 * (t >> 1) + 2 * (t & 1) + k : 384 + k) * 3584;
            tg[t][k] = __builtin_amdgcn_raw_buffer_load_b128(rg, t < 6 ? vo : vo6, so, AUX_FFN);
            tu[t][k] = __builtin_amdgcn_raw_buffer_load_b128(ru, t < 6 ? vo : vo6, so, AUX_FFN);
          }
        const u32 vd = lane < 56 ? lane * 16 : 0x80000000u;
#pragma unroll
        for (int t = 0; t < 8; ++t)
#pragma unroll
          for (int k = 0; k < 2; ++k) td[t][k] = __builtin_amdgcn_raw_buffer_load_b128(rd, vd, (fg * 16 + 2 * t + k) * 896, AUX_FFN);
        SCHED();
#pragma unroll
        for (int t = 0; t < 7; ++t)
#pragma unroll
          for (int q = 0; q < 4; ++q) {
            wgp[t][2 * q] = __builtin_amdgcn_perm(tg[t][1][q], tg[t][0][q], 0x05040100); wgp[t][2 * q + 1] = __builtin_amdgcn_perm(tg[t][1][q], tg[t][0][q], 0x07060302);
            wup[t][2 * q] = __builtin_amdgcn_perm(tu[t][1][q], tu[t][0][q], 0x05040100); wup[t][2 * q + 1] = __builtin_amdgcn_perm(tu[t][1][q], tu[t][0][q], 0x07060302);
            asm volatile("" : "+v"(wgp[t][2 * q]), "+v"(wgp[t][2 * q + 1]), "+v"(wup[t][2 * q]), "+v"(wup[t][2 * q + 1]));
          }
#pragma unroll
        for (int t = 0; t < 8; ++t)
#pragma unroll
          for (int q = 0; q < 4; ++q) {
            wdp[t][2 * q] = __builtin_amdgcn_perm(td[t][1][q], td[t][0][q], 0x05040100); wdp[t][2 * q + 1] = __builtin_amdgcn_perm(td[t][1][q], td[t][0][q], 0x07060302);
            asm volatile("" : "+v"(wdp[t][2 * q]), "+v"(wdp[t][2 * q + 1]));
          }
      }
      ST(11);
      wait_prog(L, G_X2, lane);
      poll64(p, WS_F2 + (b >> 4) * 16 * 128, ep, lane);
      ST(12);
      BAR();
      BAR();
      BAR();
      ST(13);
      const u32 key = poll_keys(p, ep, lane);
      ST(14);
      const u32 cb = WS_CAND + ((2047 - (key & 0x7ff)) >> 3) * 3584;
      u32 hp[7];
      _Pragma("unroll 1") for (int sp = 0; sp < SPIN_G; ++sp) {
        FENCE();
        v4u q[3];
#pragma unroll
        for (int i = 0; i < 3; ++i) q[i] = __builtin_amdgcn_raw_buffer_load_b128(rw, (lane >> 1) * 16 + i * 512, cb, SC1);
        const v2u q6 = __builtin_amdgcn_raw_buffer_load_b64(rw, (lane >> 1) * 8 + 1536, cb, SC1);
        bool ok = q6.y == ep;
#pragma unroll
        for (int i = 0; i < 3; ++i) { ok &= q[i].y == ep && q[i].w == ep; hp[2 * i] = q[i].x; hp[2 * i + 1] = q[i].z; }
        hp[6] = q6.x;
        if (__all(ok)) break;
      }
      FENCE();
      ST(15);
      float g[8], u[8];
#pragma unroll
      for (int n = 0; n < 8; ++n) { g[n] = 0.f; u[n] = 0.f; }
#pragma unroll
      for (int t = 0; t < 7; ++t)
#pragma unroll
        for (int n = 0; n < 8; ++n) { g[n] = dot2(wgp[t][n], hp[t], g[n]); u[n] = dot2(wup[t][n], hp[t], u[n]); }
      const float g0 = h16(h32(g[0], g[4]), h32(g[2], g[6])), g1 = h16(h32(g[1], g[5]), h32(g[3], g[7]));
      const float u0 = h16(h32(u[0], u[4]), h32(u[2], u[6])), u1 = h16(h32(u[1], u[5]), h32(u[3], u[7]));
      const bool b2 = lane & 4;
      const float gr1 = dppf<SHR4>(g1), gl0 = dppf<SHL4>(g0), ur1 = dppf<SHR4>(u1), ul0 = dppf<SHL4>(u0);
      float gg = b2 ? g1 + gr1 : g0 + gl0;
      float uu = b2 ? u1 + ur1 : u0 + ul0;
      gg += dppf<ROR8>(gg); uu += dppf<ROR8>(uu); gg += dppf<XOR2>(gg); uu += dppf<XOR2>(uu);
      const float act = rbf(siluf(rbf(gg))) * rbf(uu);
      const u32 ap = pk(act, dppf<SHL4>(act));
      float o[8];
#pragma unroll
      for (int n = 0; n < 8; ++n) o[n] = 0.f;
#pragma unroll
      for (int t = 0; t < 8; ++t) {
        const u32 s = __builtin_amdgcn_readlane(ap, (t >> 2) + ((t >> 1) & 1) * 32 + (t & 1) * 16);
#pragma unroll
        for (int n = 0; n < 8; ++n) o[n] = dot2(wdp[t][n], s, o[n]);
      }
      if (lane < 56) {
#pragma unroll
        for (int s = 0; s < 2; ++s) {
          const v4u gr = {pk(o[4 * s], o[4 * s + 1]), ep, pk(o[4 * s + 2], o[4 * s + 3]), ep};
          bstp<0>(gr, (lane >> 2) * 2 * 7168 + (lane & 3) * 16, rw, WS_FP + s * 7168 + fg * 64);
        }
      }
      ST(16);
      STEND(17);
    } else {
      wait_prog(L, G_X2, lane);
      poll64(p, WS_F2 + (b >> 4) * 16 * 128, ep, lane);
      ST(10);
      BAR();
      BAR();
      BAR();
      if (b < NFFN + NFIN) {
        const int f = b - NFFN, col = 32 * (f >> 1) + 8 * (lane & 3) + 4 * (f & 1);
        const v2u gb = ldsld<v2u>(L, L_MOD + 5 * 896 + col * 2);
        ST(13);
        const u32 key = poll_keys(p, ep, lane);
        ST(20);
        const u32 row = 2047 - (key & 0x7ff), kb = (key & ~0x7ffu) | 0x400u;
        const float rv = __uint_as_float((kb & 0x80000000u) ? (kb & 0x7fffffffu) : ~kb);
        const float sg = rbf(__builtin_amdgcn_rcpf(1.f + __expf(-rbf(rv))));
        v4u z;
        _Pragma("unroll 1") for (int sp = 0; sp < SPIN_G; ++sp) {
          FENCE();
          z = __builtin_amdgcn_raw_buffer_load_b128(rw, col * 4, WS_CAND + (row >> 3) * 3584 + 1792, SC1);
          const u32 dn = __builtin_amdgcn_raw_buffer_load_b32(rw, 0, WS_DONE + (row >> 3) * 128, SC1);
          if (__all(z.y == ep && z.w == ep && dn == ep)) break;
        }
        ST(21);
        float s0, s1, s2, s3;
        _Pragma("unroll 1") for (int sp = 0; sp < SPIN_G; ++sp) {
          FENCE();
          v4u q[7];
#pragma unroll
          for (int i = 0; i < 7; ++i) q[i] = __builtin_amdgcn_raw_buffer_load_b128(rw, lane * 16, WS_FP + f * 7168 + i * 1024, SC1);
          bool ok = true; s0 = s1 = s2 = s3 = 0.f;
#pragma unroll
          for (int i = 0; i < 7; ++i) { ok &= q[i].y == ep && q[i].w == ep; s0 += lo16(q[i].x); s1 += hi16(q[i].x); s2 += lo16(q[i].z); s3 += hi16(q[i].z); }
          if (__all(ok)) break;
        }
        FENCE();
        ST(22);
        float a0 = h32(s0, s2), a1 = h32(s1, s3);
        a0 = h16(a0, a0); a1 = h16(a1, a1);
        a0 += dppf<ROR4>(a0); a1 += dppf<ROR4>(a1); a0 += dppf<ROR8>(a0); a1 += dppf<ROR8>(a1);
        const u32 zz = lane < 32 ? z.x : z.z, gg = lane < 32 ? gb.x : gb.y;
        const u32 o = pk(lo16(zz) + lo16(gg) * (rbf(a0) * sg), hi16(zz) + hi16(gg) * (rbf(a1) * sg));
        if ((lane & 28) == 0) __builtin_amdgcn_raw_buffer_store_b32(o, mk(p.out, 2048 * 896), col * 2 + (lane >> 5) * 4, row * 896, SC1);
        if (f == 0 && lane == 0) __builtin_amdgcn_raw_buffer_store_b32(ep, rw, 0, WS_EPOCH, SC1);
        ST(23);
        STEND(24);
      } else {
        STEND(13);
      }
    }
#pragma unroll
    for (int i = 0; i < 8; ++i) asm volatile("" : "=v"(n[i][0]), "=v"(n[i][1]), "=v"(n[i][2]), "=v"(n[i][3]));
    asm volatile("s_endpgm" : "=v"(ha.a[0]), "=v"(ha.a[1]), "=v"(ha.a[2]), "=v"(ha.a[3]), "=v"(ha.m)::"memory");
  }
  if (w < 4) {
    warm_done(sink);
    wait_words(L, L_CTL + C_WD + (lane & 7) * 4, 1);
    ST(4);
    v4u wf[2][14];
    {
      const int r = lane & 15, g = lane >> 4;
      const u32 l0 = w * 28672 + (r >> 3) * 7168 + (r & 7) * 128 + ((g ^ (r & 7)) * 16), l1 = l0 ^ 64;
#pragma unroll
      for (int ks = 0; ks < 14; ++ks) wf[0][ks] = ldsld<v4u>(L, ((ks & 1) ? l1 : l0) + (ks >> 1) * 1024);
      const u32 d = w < 3 ? 14336 : 0;
#pragma unroll
      for (int ks = 0; ks < 14; ++ks) wf[1][ks] = ldsld<v4u>(L, ((ks & 1) ? l1 : l0) + d + (ks >> 1) * 1024);
#pragma unroll
      for (int ks = 0; ks < 14; ++ks) asm volatile("" : "+v"(wf[0][ks]), "+v"(wf[1][ks]));
    }
    const u32 oob = 0x80000000u;
    u32 pw = L_CTL + w * 4;
    asm volatile("" : "+v"(pw));
    u32 l0, l1, vo0 = lane * 16, vo1 = w < 3 ? vo0 : oob, so0 = WS_AB + ((half * 64 + sig * 2) * 28 + ec * 7 + 2 * w) * 1024, so1 = so0 + 28 * 1024;
    u32 vf1 = lane == 0 ? WS_F1 + ((half * 4 + ec) * 32 + sig) * 128 + w * 4 : oob, vf2 = lane == 0 ? WS_F2 + (tt * 16 + (half * 2 + kh) * 4 + ec) * 128 + w * 4 : oob, vep = ep;
    { const int r = lane & 15, g = lane >> 4; l0 = L_H + (r >> 3) * 7168 + (r & 7) * 128 + ((g ^ (r & 7)) * 16); l1 = l0 ^ 64; }
    asm volatile("" : "+v"(l0), "+v"(l1), "+v"(vo0), "+v"(vo1), "+v"(vf1), "+v"(vf2), "+v"(vep));
    asm volatile("" : "+s"(so0), "+s"(so1));
    ST(5);
    BAR();
    ST(6);
    h_write(n, lane, ha);
    BAR();
    ST(7);
    small_gemm(rw, wf, l0, l1, vo0, vo1, so0, so1);
    marki<C_PROG, G_H>(pw);
    ST(8);
    if (S3U >= 0) warm_rows<8, S3U, S3U + 1>(sink, aq + w * 32768);
    flag_after_drain(rw, vf1, vep);
    if (W3CTL == G_X1) mark<C_PROG>(pw, G_X1);
    tail_issue(p, b, w, lane, ti);
    ST(9);
    warm_done(sink);
    const u32 bs = WS_AB + ((half * 64 + kh * 32) * 28 + ec * 7) * 1024;
    const u32 ps = WS_PART + tt * 16 * 28672 + (half * 2 + kh) * 7168 + ec * 7 * 256;
    if (w == 0) big_gemm<0>(p, L, lane, bs, ps);
    else if (w == 1) big_gemm<1>(p, L, lane, bs, ps);
    else if (w == 2) big_gemm<2>(p, L, lane, bs, ps);
    else big_gemm<3>(p, L, lane, bs, ps);
    ST(26);
    flag_after_drain(rw, vf2, vep);
    mark<C_PROG>(pw, G_X2);
    ST(11);
  } else {
    warm_done(sink);
    ST(4);
    BAR();
    h_write(n, lane, ha);
    BAR();
    ST(6);
    if (w == 4) {
      const rsrc_t ra = mk(half ? p.sim : p.sre, 2048 * 4096);
      const u32 r7 = lane >> 3, sa0 = tt * 128 * 4096 + kh * 2048, sa9 = sa0 + 9 * 128;
      u32 va[16];
#pragma unroll
      for (int pi = 0; pi < 16; ++pi) {
        va[pi] = (8 * (r7 >> 2) + (r7 & 3) + 32 * (pi >> 2) + 16 * (pi & 1) + 4 * ((pi >> 1) & 1)) * 4096 + (((lane & 7) ^ r7) * 16);
        asm volatile("" : "+v"(va[pi]));
      }
      auto slices = [&](auto K0, auto K1) {
        static_for<K0.value * 16, K1.value * 16>([&](auto I) { constexpr int k = I.value >> 4, pi = I.value & 15; dmac<(k % 9) * 16384 + pi * 1024, (k % 9) * 128>(va[pi], ra, k < 9 ? sa0 : sa9); });
      };
      slices(IC<0>{}, IC<NPRE>{});
      ST(7);
      wait_prog(L, W3CTL, lane);
      ST(8);
      slices(IC<NPRE>{}, IC<9>{});
      tail_issue(p, b, w, lane, ti);
      ST(9);
      VM0();
      ST(10);
      BAR();
      static_for<0, 4>([&](auto Q) {
        constexpr int q = Q.value;
        wait_prog(L, G_RING + q, lane);
        if (!XNOA) slices(IC<9 + 2 * q>{}, IC<(q < 3 ? 11 + 2 * q : 16)>{});
        VM0();
        mark<C_FULL>(L_CTL, q + 1);
        ST(11 + q);
      });
    } else {
      tail_issue(p, b, w, lane, ti);
      constexpr int SM = S2A + 2 < S2B ? S2A + 2 : S2B;
      warm_rows<16, S2A, SM>(sink, aq + (w - 5) * 65536);
      ST(7);
      asm volatile("s_barrier" ::: "memory");
      warm_rows<16, SM, S2B>(sink, aq + (w - 5) * 65536);
      warm_done(sink);
      asm volatile("s_waitcnt vmcnt(0)" : "+v"(ti.xa), "+v"(ti.xb), "+v"(ti.wr)::"memory");
      ST(8);
    }
  }

  {
    const int ci = lane & 3, e0 = 64 * w + 4 * (lane >> 2);
    const v2u xa = ti.xa, xb = ti.xb, wr = ti.wr;
    const v2u ga = ldsld<v2u>(L, L_MOD + 2 * 896 + e0 * 2), sb = ldsld<v2u>(L, L_MOD + 3 * 896 + e0 * 2), cb = ldsld<v2u>(L, L_MOD + 4 * 896 + e0 * 2), gb = ldsld<v2u>(L, L_MOD + 5 * 896 + e0 * 2);
    float gaf[4] = {lo16(ga.x), hi16(ga.x), lo16(ga.y), hi16(ga.y)}, sbf[4] = {lo16(sb.x), hi16(sb.x), lo16(sb.y), hi16(sb.y)};
    float cbf[4] = {lo16(cb.x), hi16(cb.x), lo16(cb.y), hi16(cb.y)}, gbf[4] = {lo16(gb.x), hi16(gb.x), lo16(gb.y), hi16(gb.y)};
    float wrf[4] = {lo16(wr.x), hi16(wr.x), lo16(wr.y), hi16(wr.y)};
    float va[4] = {lo16(xa.x), hi16(xa.x), lo16(xa.y), hi16(xa.y)}, vb[4] = {lo16(xb.x), hi16(xb.x), lo16(xb.y), hi16(xb.y)};
    float wp[4], Wp = 0.f, Cp = 0.f;
#pragma unroll
    for (int i = 0; i < 4; ++i) { wp[i] = cbf[i] * wrf[i]; Wp += wp[i]; Cp += sbf[i] * wrf[i]; }
#pragma unroll
    for (int i = 0; i < 4; ++i) asm volatile("" : "+v"(wp[i]), "+v"(va[i]), "+v"(vb[i]), "+v"(gaf[i]));
    asm volatile("" : "+v"(Wp), "+v"(Cp));
    ST(20);
    BAR();
    ST(21);
    v4u q[4];
#pragma unroll
    for (int s = 0; s < 4; ++s) q[s] = __builtin_amdgcn_raw_buffer_load_b128(rw, lane * 16, WS_PART + b * 28672 + s * 7168 + w * 1024, SC1);
#pragma unroll
    for (int i = 0; i < 2; ++i) {
      va[2 * i] += gaf[2 * i] * ((lo16(q[0][i]) + lo16(q[1][i])) - (lo16(q[2][i]) + lo16(q[3][i])));
      va[2 * i + 1] += gaf[2 * i + 1] * ((hi16(q[0][i]) + hi16(q[1][i])) - (hi16(q[2][i]) + hi16(q[3][i])));
      vb[2 * i] += gaf[2 * i] * ((lo16(q[0][2 + i]) + lo16(q[1][2 + i])) - (lo16(q[2][2 + i]) + lo16(q[3][2 + i])));
      vb[2 * i + 1] += gaf[2 * i + 1] * ((hi16(q[0][2 + i]) + hi16(q[1][2 + i])) - (hi16(q[2][2 + i]) + hi16(q[3][2 + i])));
    }
    ST(22);
    float s1a = (va[0] + va[1]) + (va[2] + va[3]), s1b = (vb[0] + vb[1]) + (vb[2] + vb[3]);
    float s2a = (va[0] * va[0] + va[1] * va[1]) + (va[2] * va[2] + va[3] * va[3]), s2b = (vb[0] * vb[0] + vb[1] * vb[1]) + (vb[2] * vb[2] + vb[3] * vb[3]);
    float s3a = (va[0] * wp[0] + va[1] * wp[1]) + (va[2] * wp[2] + va[3] * wp[3]), s3b = (vb[0] * wp[0] + vb[1] * wp[1]) + (vb[2] * wp[2] + vb[3] * wp[3]);
    float v0 = h16(h32(s1a, s1b), h32(s2a, s2b)), v1 = h16(h32(s3a, s3b), h32(Wp, Cp));
    v0 += dppf<ROR4>(v0); v1 += dppf<ROR4>(v1); v0 += dppf<ROR8>(v0); v1 += dppf<ROR8>(v1);
    if ((lane & 12) == 0) {
      const u32 o = L_ST + ((lane >> 4) & 1) * 256 + w * 32 + (lane >> 5) * 16 + ci * 4;
      ldsst<float>(L, o, v0); ldsst<float>(L, o + 512, v1);
    }
    BAR();
    float t[4];
#pragma unroll
    for (int k = 0; k < 4; ++k) { t[k] = ldsld<float>(L, L_ST + k * 256 + lane * 4); t[k] = lane < 56 ? t[k] : 0.f; }
#pragma unroll
    for (int k = 0; k < 4; ++k) { t[k] += dppf<ROR8>(t[k]); t[k] = h16(t[k], t[k]); t[k] = h32(t[k], t[k]); }
    const bool up = lane & 4;
    const float oth = dppf<ROR4>(t[3]), Wt = up ? oth : t[3], Ct = up ? t[3] : oth;
    const float mu = t[0] * (1.f / 448.f);
    const float rs = __builtin_amdgcn_rsqf(fmaxf(t[1] * (1.f / 448.f) - mu * mu, 0.f) + 1e-5f);
    const float rr = rs * (t[2] - mu * Wt) + Ct;
    u32 key = __float_as_uint(rr);
    key = (key & 0x80000000u) ? ~key : (key | 0x80000000u);
    key = (key & ~0x7ffu) | (2047 - (b * 8 + (lane & 7)));
    key = umax(key, dppu<XOR1>(key)); key = umax(key, dppu<XOR2>(key)); key = umax(key, dppu<HMIR>(key));
    if (w == 0 && lane == 0) __builtin_amdgcn_raw_buffer_store_b64(v2u{key, ep}, rw, 0, WS_KEY + b * 128, SC1);
    ST(23);
    const float mo = dppf<ROR4>(mu), ro = dppf<ROR4>(rs);
    const float mua = up ? mo : mu, mub = up ? mu : mo, rsa = up ? ro : rs, rsb = up ? rs : ro;
    float ha[4], hb[4];
#pragma unroll
    for (int i = 0; i < 4; ++i) { ha[i] = (va[i] - mua) * rsa * cbf[i] + sbf[i]; hb[i] = (vb[i] - mub) * rsb * cbf[i] + sbf[i]; }
    ldsst<v2u>(L, L_H2 + ci * 896 + e0 * 2, v2u{pk(ha[0], ha[1]), pk(ha[2], ha[3])});
    ldsst<v2u>(L, L_H2 + (ci + 4) * 896 + e0 * 2, v2u{pk(hb[0], hb[1]), pk(hb[2], hb[3])});
    ldsst<v2u>(L, L_Z + ci * 896 + e0 * 2, v2u{pk(va[0] + gbf[0] * ha[0], va[1] + gbf[1] * ha[1]), pk(va[2] + gbf[2] * ha[2], va[3] + gbf[3] * ha[3])});
    ldsst<v2u>(L, L_Z + (ci + 4) * 896 + e0 * 2, v2u{pk(vb[0] + gbf[0] * hb[0], vb[1] + gbf[1] * hb[1]), pk(vb[2] + gbf[2] * hb[2], vb[3] + gbf[3] * hb[3])});
    BAR();
    if (w == 0) {
      const rsrc_t ro_ = mk(p.out, 2048 * 896);
      v4u z[7];
#pragma unroll
      for (int i = 0; i < 7; ++i) z[i] = ldsld<v4u>(L, L_Z + i * 1024 + lane * 16);
      static_for<0, 7>([&](auto I) { bstp<(I.value & 3) * 1024>(z[I.value], lane * 16, ro_, b * 7168 + (I.value >> 2) * 4096); });
      VM0();
      if (lane == 0) __builtin_amdgcn_raw_buffer_store_b32(ep, rw, 0, WS_DONE + b * 128, SC1);
    } else if (w < 3) {
      const u32 best = (2047 - (__builtin_amdgcn_readfirstlane(key) & 0x7ff)) & 7;
      const u32 src = (w == 1 ? L_H2 : L_Z) + best * 896;
#pragma unroll
      for (int k = 0; k < 2; ++k) {
        if (k * 64 + lane < 112) {
          const v2u d = ldsld<v2u>(L, src + (k * 64 + lane) * 8);
          bstp<0>(v4u{d.x, ep, d.y, ep}, lane * 16 + k * 1024, rw, WS_CAND + b * 3584 + (w - 1) * 1792);
        }
      }
    }
    ST(24);
    STEND(25);
  }
}

extern "C" void kernel_launch(void* const* d_in, const int* in_sizes, int n_in, void* d_out, int out_size, void* d_ws, size_t ws_size, hipStream_t stream) {
  Params p;
  p.x = (const u16*)d_in[0]; p.sre = (const u16*)d_in[1]; p.sim = (const u16*)d_in[2]; p.dre = (const u16*)d_in[3]; p.dim = (const u16*)d_in[4];
  p.wr = (const u16*)d_in[6]; p.wg = (const u16*)d_in[7]; p.wu = (const u16*)d_in[8]; p.wd = (const u16*)d_in[9]; p.c = (const u16*)d_in[10]; p.wmod = (const u16*)d_in[11];
  p.out = (u16*)d_out; p.ws = (unsigned char*)d_ws;
  (void)hipFuncSetAttribute((const void*)k_fused, hipFuncAttributeMaxDynamicSharedMemorySize, L_TOTAL);
  k_fused<<<256, 512, L_TOTAL, stream>>>(p);
}
```

```cpp
#include <hip/hip_runtime.h>
#include <stdint.h>

typedef unsigned short u16;
typedef unsigned u32;
typedef unsigned long long u64;
typedef u32 v4u __attribute__((ext_vector_type(4)));
typedef u32 v2u __attribute__((ext_vector_type(2)));
typedef float f32x4 __attribute__((ext_vector_type(4)));
typedef float f32x2 __attribute__((ext_vector_type(2)));
typedef __bf16 bf16x2 __attribute__((ext_vector_type(2)));
typedef __amdgpu_buffer_rsrc_t rsrc_t;
#define LAS __attribute__((address_space(3)))
#define DI __device__ __forceinline__

#define AUX_FFN 0
#define PD 6
#define PSLEEP 1
#define TA 0
#define TB 8
#define NP0 4
#define LAG 2
#define FTS 5
#define R1 3
#define XE 2
#define XROT 1
#define HPRIO 0
#define WSWAP 1
#define NPRE 0
#define W3CTL G_H
#define FFNKS (32 - PD)
#define SC1 16
#define XNOB 0
#define XNOA 0
#define XNOF 0

static constexpr u32 WS_EPOCH = 0;
static constexpr u32 WS_MOD = 4096, MODREP = 10880;
static constexpr u32 WS_F1 = 98304, WS_F2 = 131072, WS_KEY = 163840, WS_DONE = 196608;
static constexpr u32 WS_CAND = 262144;
static constexpr u32 WS_FP = 1310720;
static constexpr u32 WS_AB = 2097152;
static constexpr u32 WS_PART = 6291456;
static constexpr u32 WS_STAMP = 14u << 20;
static constexpr u32 WS_SIZE = 16u << 20;
static constexpr u32 L_H = 100352;
static constexpr u32 L_MOD = 157696;
static constexpr u32 L_CTL = L_MOD + 5376;
static constexpr u32 L_RED = L_CTL + 128;
static constexpr u32 L_TOTAL = 163840;
static constexpr u32 C_PROG = 0, C_FULL = 16, C_X1 = 20, C_WD = 32, C_H2 = 64;
static constexpr u32 G_H = 1, G_X1 = 2, G_RING = 3, G_FFN = 10, G_X2 = 11;
static constexpr u32 L_ST = 0, L_Z = 1024, L_H2 = 8192;
static constexpr int SPIN_G = 1 << 14, SPIN_L = 1 << 18;
static constexpr int NFFN = 112, NFIN = 28;

struct Params {
  const u16 *x, *sre, *sim, *dre, *dim, *wr, *wg, *wu, *wd, *c, *wmod;
  u16* out;
  unsigned char* ws;
};

template <int V> struct IC { static constexpr int value = V; };
template <int A, int B, typename F> DI void static_for(F f) { if constexpr (A < B) { f(IC<A>{}); static_for<A + 1, B>(f); } }
DI rsrc_t mk(const void* p, u32 n) { return __builtin_amdgcn_make_buffer_rsrc((void*)p, 0, n, 0x00020000); }
DI float lo16(u32 v) { return __uint_as_float(v << 16); }
DI float hi16(u32 v) { return __uint_as_float(v & 0xffff0000u); }
DI u32 pk(float a, float b) { f32x2 v = {a, b}; return __builtin_bit_cast(u32, __builtin_convertvector(v, bf16x2)); }
DI float rbf(float a) { return lo16(pk(a, a)); }
DI float dot2(u32 a, u32 b, float c) { return __builtin_amdgcn_fdot2_f32_bf16(__builtin_bit_cast(bf16x2, a), __builtin_bit_cast(bf16x2, b), c, false); }
template <int CTRL> DI float dppf(float v) { return __uint_as_float(__builtin_amdgcn_update_dpp(0, __float_as_uint(v), CTRL, 0xf, 0xf, true)); }
template <int CTRL> DI u32 dppu(u32 v) { return __builtin_amdgcn_update_dpp(0, v, CTRL, 0xf, 0xf, true); }
static constexpr int XOR1 = 0xB1, XOR2 = 0x4E, HMIR = 0x141, MIR = 0x140, ROR4 = 0x124, ROR8 = 0x128, SHL4 = 0x104, SHR4 = 0x114;
DI float h32(float a, float b) { auto r = __builtin_amdgcn_permlane32_swap(__float_as_uint(a), __float_as_uint(b), false, false); return __uint_as_float(r[0]) + __uint_as_float(r[1]); }
DI float h16(float a, float b) { auto r = __builtin_amdgcn_permlane16_swap(__float_as_uint(a), __float_as_uint(b), false, false); return __uint_as_float(r[0]) + __uint_as_float(r[1]); }
DI u32 max32(u32 a) { auto r = __builtin_amdgcn_permlane32_swap(a, a, false, false); return r[0] > r[1] ? r[0] : r[1]; }
DI u32 max16(u32 a) { auto r = __builtin_amdgcn_permlane16_swap(a, a, false, false); return r[0] > r[1] ? r[0] : r[1]; }
DI u32 umax(u32 a, u32 b) { return a > b ? a : b; }
DI float rdl(float v, int l) { return __uint_as_float(__builtin_amdgcn_readlane(__float_as_uint(v), l)); }
DI float siluf(float v) { return v * __builtin_amdgcn_rcpf(1.f + __expf(-v)); }

#define BAR() asm volatile("s_waitcnt lgkmcnt(0)\n\ts_barrier" ::: "memory")
#define VM0() asm volatile("s_waitcnt vmcnt(0)" ::: "memory")
#define FENCE() asm volatile("" ::: "memory")
#define SCHED() __builtin_amdgcn_sched_barrier(0)
#define MFMA(acc, a, b) asm volatile("v_mfma_f32_16x16x32_bf16 %0, %1, %2, %0" : "+v"(acc) : "v"(a), "v"(b))
#define MFMA0(acc, a, b) asm volatile("v_mfma_f32_16x16x32_bf16 %0, %1, %2, 0" : "=&v"(acc) : "v"(a), "v"(b))

template <int DST, int IMM> DI void dmac(u32 voff, rsrc_t rs, u32 soff) {
  asm volatile("s_mov_b32 m0, %3\n\ts_nop 0\n\tbuffer_load_dwordx4 %0, %1, %2 offen offset:%4 lds" ::"v"(voff), "s"(rs), "s"(soff), "i"(DST - IMM), "i"(IMM) : "memory");
}
template <int DC, int IMM> DI void dmar(u32 voff, rsrc_t rs, u32 soff, u32 db) {
  asm volatile("s_add_u32 m0, %3, %4\n\ts_nop 0\n\tbuffer_load_dwordx4 %0, %1, %2 offen offset:%5 lds" ::"v"(voff), "s"(rs), "s"(soff), "s"(db), "i"(DC - IMM), "i"(IMM) : "memory", "scc");
}
template <int OFF> DI void touch(u32& sink, u32 vo, rsrc_t rs, u32 so) { asm volatile("buffer_load_dword %0, %1, %2, %3 offen offset:%4" : "+v"(sink) : "v"(vo), "s"(rs), "s"(so), "i"(OFF) : "memory"); }
template <int V> DI void wait_v() { asm volatile("s_waitcnt vmcnt(%0)" ::"i"(V) : "memory"); }
template <int OFF> DI void dsrd(v4u& d, u32 a) { asm volatile("ds_read_b128 %0, %1 offset:%2" : "=v"(d) : "v"(a), "i"(OFF)); }
template <int OFF> DI void bld(v4u& d, u32 vo, rsrc_t rs, u32 so) { asm volatile("buffer_load_dwordx4 %0, %1, %2, %3 offen offset:%4 sc1" : "=v"(d) : "v"(vo), "s"(rs), "s"(so), "i"(OFF)); }
template <int OFF> DI void bst(v4u d, u32 vo, rsrc_t rs, u32 so) { asm volatile("buffer_store_dwordx4 %0, %1, %2, %3 offen offset:%4 sc1" ::"v"(d), "v"(vo), "s"(rs), "s"(so), "i"(OFF) : "memory"); }
template <int OFF> DI void bstp(v4u d, u32 vo, rsrc_t rs, u32 so) { asm volatile("buffer_store_dwordx4 %0, %1, %2, %3 offen offset:%4 sc1\n\ts_nop 1" ::"v"(d), "v"(vo), "s"(rs), "s"(so), "i"(OFF) : "memory"); }
DI u32 cvt2(float a, float b) { u32 r; asm volatile("v_cvt_pk_bf16_f32 %0, %1, %2" : "=v"(r) : "v"(a), "v"(b)); return r; }
template <int V, int G> DI void wait_vl() { asm volatile("s_waitcnt vmcnt(%0) lgkmcnt(%1)" ::"i"(V), "i"(G)); }
template <bool Z> DI void mfma(f32x4& acc, const v4u& a, const v4u& b) { if constexpr (Z) MFMA0(acc, a, b); else MFMA(acc, a, b); }
template <int OFF> DI void dswr(u32 a, v4u d) { asm volatile("ds_write_b128 %0, %1 offset:%2" ::"v"(a), "v"(d), "i"(OFF) : "memory"); }
template <int OFF> DI void mark(u32 a, u32 v) { asm volatile("ds_write_b32 %0, %1 offset:%2" ::"v"(a), "v"(v), "i"(OFF) : "memory"); }
template <int OFF, int V> DI void marki(u32 a) { u32 t; asm volatile("v_mov_b32 %0, %3\n\tds_write_b32 %1, %0 offset:%2" : "=&v"(t) : "v"(a), "i"(OFF), "i"(V) : "memory"); }
template <int G> DI void wait_l() { asm volatile("s_waitcnt lgkmcnt(%0)" ::"i"(G)); }

#define ST0() do { } while (0)
#define ST(prev) do { } while (0)
#define STEND(prev) do { } while (0)

template <typename T> DI T ldsld(LAS unsigned char* L, u32 off) { return *(LAS T*)(L + off); }
template <typename T> DI void ldsst(LAS unsigned char* L, u32 off, T v) { *(LAS T*)(L + off) = v; }
DI u32 lds_inc(LAS unsigned char* L, u32 off) { return __hip_atomic_fetch_add((LAS u32*)(L + off), 1u, __ATOMIC_RELAXED, __HIP_MEMORY_SCOPE_WORKGROUP); }
DI u32 lds_peek(LAS unsigned char* L, u32 off) { return __hip_atomic_load((LAS u32*)(L + off), __ATOMIC_RELAXED, __HIP_MEMORY_SCOPE_WORKGROUP); }
DI u32 lds_wait_ge(LAS unsigned char* L, u32 off, u32 v) {
  u32 r = 0;
  _Pragma("unroll 1") for (int sp = 0; sp < SPIN_L; ++sp) {
    FENCE();
    r = __builtin_amdgcn_readfirstlane(lds_peek(L, off));
    if (r >= v) break;
  }
  FENCE();
  return r;
}

DI void wait_words(LAS unsigned char* L, u32 off, u32 v) {
  _Pragma("unroll 1") for (int sp = 0; sp < SPIN_L; ++sp) {
    FENCE();
    if (__all(lds_peek(L, off) >= v)) break;
    __builtin_amdgcn_s_sleep(1);
  }
  FENCE();
}

template <int I0, int I1> DI void x_issue(const Params& p, u32 vo, const u32 (&so)[8], v4u (&xr)[8]) {
  const rsrc_t rx = mk(p.x, 2048 * 896);
#pragma unroll
  for (int i = I0; i < I1; ++i) asm volatile("buffer_load_dwordx4 %0, %1, %2, %3 offen" : "=v"(xr[i]) : "v"(vo), "s"(rx), "s"(so[i]) : "memory");
}
template <typename H>
DI void ln_stats(v4u (&xr)[8], int lane, f32x2 (&n)[8][4], H hook) {
  float s[8], q[8];
  const u32 one2 = 0x3f803f80u;
  static_for<0, 8>([&](auto I) {
    constexpr int i = I.value;
    float a, c;
    { v4u t = xr[i]; asm volatile("s_waitcnt vmcnt(%1)" : "+v"(t) : "i"(7 - i + NP0 + (i < 12 - NP0 ? i : 12 - NP0)) : "memory"); xr[i] = t; }
    if constexpr (NP0 + i < 12) hook(IC<NP0 + i>{});
    asm volatile("v_dot2_f32_bf16 %0, %2, %6, 0\n\tv_dot2_f32_bf16 %1, %2, %2, 0\n\tv_dot2_f32_bf16 %0, %3, %6, %0\n\tv_dot2_f32_bf16 %1, %3, %3, %1\n\t"
                 "v_dot2_f32_bf16 %0, %4, %6, %0\n\tv_dot2_f32_bf16 %1, %4, %4, %1\n\tv_dot2_f32_bf16 %0, %5, %6, %0\n\tv_dot2_f32_bf16 %1, %5, %5, %1"
                 : "=&v"(a), "=&v"(c) : "v"(xr[i][0]), "v"(xr[i][1]), "v"(xr[i][2]), "v"(xr[i][3]), "s"(one2));
    s[i] = a; q[i] = c;
  });
  ST(28);
  asm volatile("s_nop 2" : "+v"(s[0]), "+v"(s[1]), "+v"(s[2]), "+v"(s[3]), "+v"(s[4]), "+v"(s[5]), "+v"(s[6]), "+v"(s[7]),
                           "+v"(q[0]), "+v"(q[1]), "+v"(q[2]), "+v"(q[3]), "+v"(q[4]), "+v"(q[5]), "+v"(q[6]), "+v"(q[7]));
  const float s0 = h16(h32(s[0], s[4]), h32(s[2], s[6])), s1 = h16(h32(s[1], s[5]), h32(s[3], s[7]));
  const float q0 = h16(h32(q[0], q[4]), h32(q[2], q[6])), q1 = h16(h32(q[1], q[5]), h32(q[3], q[7]));
  const bool up = lane & 8;
  float sv = (up ? s1 : s0) + dppf<ROR8>(up ? s0 : s1), qv = (up ? q1 : q0) + dppf<ROR8>(up ? q0 : q1);
  sv += dppf<XOR1>(sv); qv += dppf<XOR1>(qv); sv += dppf<XOR2>(sv); qv += dppf<XOR2>(qv); sv += dppf<HMIR>(sv); qv += dppf<HMIR>(qv);
  const float mu = sv * (1.f / 448.f);
  const float rs = __builtin_amdgcn_rsqf(fmaxf(qv * (1.f / 448.f) - mu * mu, 0.f) + 1e-5f), nm = -mu * rs;
  f32x2 rn[8];
#pragma unroll
  for (int i = 0; i < 8; ++i) { const int l = (i >> 2) * 32 + ((i >> 1) & 1) * 16 + (i & 1) * 8; rn[i] = f32x2{rdl(rs, l), rdl(nm, l)}; }
  asm volatile("s_nop 1" : "+s"(rn[0]), "+s"(rn[1]), "+s"(rn[2]), "+s"(rn[3]), "+s"(rn[4]), "+s"(rn[5]), "+s"(rn[6]), "+s"(rn[7]));
  static_for<0, 8>([&](auto I) {
    constexpr int i = I.value;
    if constexpr (NP0 + 8 + i < 12) hook(IC<NP0 + 8 + i>{});
#pragma unroll
    for (int e = 0; e < 4; ++e) {
      const f32x2 t = {lo16(xr[i][e]), hi16(xr[i][e])};
      const f32x2 r = rn[i];
      f32x2 o;
      asm volatile("v_pk_fma_f32 %0, %1, %2, %2 op_sel:[0,0,1] op_sel_hi:[1,0,1]" : "=v"(o) : "v"(t), "s"(r));
      n[i][e] = o;
    }
    SCHED();
  });
}
struct HAddr { u32 a[8], m; };
DI HAddr h_addr(int lane, int w, int rot) {
  HAddr h;
  const u32 base = L_H + ((w >> 2) * 4 + ((w >> 1) & 1)) * 7168 + (lane >> 3) * 1024 + (w & 1) * 512;
  const u32 ch = (lane & 7) ^ ((w & 1) * 4);
#pragma unroll
  for (int i = 0; i < 8; ++i) { const u32 r = (i + rot) & 7, k = r & 3; h.a[i] = base + k * 128 + (r >> 2) * 14336 + ((ch ^ k) * 16); asm volatile("" : "+v"(h.a[i])); }
  h.m = L_MOD + lane * 16;
  asm volatile("" : "+v"(h.m));
  return h;
}
struct HMod { f32x2 s[4], c[4]; };
DI void h_prep(HMod& m, const HAddr& ha) {
  v4u sa, ca;
  dsrd<0>(sa, ha.m); dsrd<896>(ca, ha.m);
  asm volatile("s_waitcnt lgkmcnt(0)" : "+v"(sa), "+v"(ca));
#pragma unroll
  for (int q = 0; q < 4; ++q) { m.s[q] = f32x2{lo16(sa[q]), hi16(sa[q])}; m.c[q] = f32x2{lo16(ca[q]), hi16(ca[q])}; asm volatile("" : "+v"(m.s[q]), "+v"(m.c[q])); }
}
DI void h_write(const f32x2 (&n)[8][4], int lane, const HAddr& ha, const HMod& m) {
  if (lane < 56) {
    const f32x2 (&s)[4] = m.s, (&c)[4] = m.c;
    f32x2 t[8][4];
    v4u h[8];
    auto F = [&](auto I) {
      constexpr int i = I.value;
      f32x2 t0 = n[i][0], t1 = n[i][1], t2 = n[i][2], t3 = n[i][3];
      const f32x2 c0 = c[0], c1 = c[1], c2 = c[2], c3 = c[3], s0 = s[0], s1 = s[1], s2 = s[2], s3 = s[3];
      asm volatile("v_pk_fma_f32 %0, %0, %4, %8\n\tv_pk_fma_f32 %1, %1, %5, %9\n\tv_pk_fma_f32 %2, %2, %6, %10\n\tv_pk_fma_f32 %3, %3, %7, %11"
                   : "+v"(t0), "+v"(t1), "+v"(t2), "+v"(t3) : "v"(c0), "v"(c1), "v"(c2), "v"(c3), "v"(s0), "v"(s1), "v"(s2), "v"(s3));
      t[i][0] = t0; t[i][1] = t1; t[i][2] = t2; t[i][3] = t3;
    };
    F(IC<0>{}); F(IC<1>{});
    static_for<0, 8>([&](auto I) {
      constexpr int i = I.value;
      const f32x2 t0 = t[i][0], t1 = t[i][1], t2 = t[i][2], t3 = t[i][3];
      u32 h0, h1, h2, h3;
      asm volatile("v_cvt_pk_bf16_f32 %0, %4, %5\n\tv_cvt_pk_bf16_f32 %1, %6, %7\n\tv_cvt_pk_bf16_f32 %2, %8, %9\n\tv_cvt_pk_bf16_f32 %3, %10, %11"
                   : "=&v"(h0), "=&v"(h1), "=&v"(h2), "=&v"(h3) : "v"(t0[0]), "v"(t0[1]), "v"(t1[0]), "v"(t1[1]), "v"(t2[0]), "v"(t2[1]), "v"(t3[0]), "v"(t3[1]));
      h[i] = v4u{h0, h1, h2, h3};
      if constexpr (i + 2 < 8) F(IC<i + 2>{});
      if constexpr (i < 6) dswr<0>(ha.a[i], h[i]);
      if constexpr (i == 7) { dswr<0>(ha.a[6], h[6]); dswr<0>(ha.a[7], h[7]); }
    });
  }
}

DI void small_gemm(LAS unsigned char* L, rsrc_t rw, const v4u (&wf)[2][14], u32 l0, u32 l1, u32 vo0, u32 vo1, u32 so0, u32 so1, u32 hw) {
  v4u a[4][2];
  static_for<0, 3>([&](auto S) { constexpr int s = S.value; dsrd<(s >> 1) * 1024>(a[s][0], (s & 1) ? l1 : l0); dsrd<14336 + (s >> 1) * 1024>(a[s][1], (s & 1) ? l1 : l0); });
  f32x4 acc[2][2][2];
  u32 o[4], hv = 0;
  static_for<0, 28>([&](auto S) {
    constexpr int st = S.value, J = st / 14, ks = st % 14, s3 = st + 3, J3 = s3 / 14, k3 = s3 % 14;
    wait_l<(st < 26 ? 4 : st == 26 ? 2 : 0)>();
    mfma<ks == 0>(acc[J][0][0], a[st & 3][0], wf[0][ks]);
    if constexpr (s3 < 28) dsrd<J3 * 28672 + (k3 >> 1) * 1024>(a[s3 & 3][0], (k3 & 1) ? l1 : l0);
    mfma<ks == 0>(acc[J][0][1], a[st & 3][0], wf[1][ks]);
    if constexpr (s3 < 28) dsrd<J3 * 28672 + 14336 + (k3 >> 1) * 1024>(a[s3 & 3][1], (k3 & 1) ? l1 : l0);
    mfma<ks == 0>(acc[J][1][0], a[st & 3][1], wf[0][ks]);
    if constexpr (st == 7) asm volatile("ds_read_b32 %0, %1" : "=v"(hv) : "v"(hw));
    if constexpr (st == 10) { asm volatile("" : "+v"(hv)); if (!__all(hv != 0)) wait_words(L, hw, 1); }
    if constexpr (st >= 16 && st < 22) {
      constexpr int n = (st - 16) / 3, ph = (st - 16) % 3;
      if constexpr (ph < 2) { o[2 * ph] = cvt2(acc[0][ph][n][0], acc[0][ph][n][1]); o[2 * ph + 1] = cvt2(acc[0][ph][n][2], acc[0][ph][n][3]); }
      else bst<n * 1024>(v4u{o[0], o[1], o[2], o[3]}, n ? vo1 : vo0, rw, so0);
    }
    mfma<ks == 0>(acc[J][1][1], a[st & 3][1], wf[1][ks]);
  });
  asm volatile("s_nop 7");
  const u32 o0 = cvt2(acc[1][0][0][0], acc[1][0][0][1]), o1 = cvt2(acc[1][0][0][2], acc[1][0][0][3]);
  const u32 o4 = cvt2(acc[1][0][1][0], acc[1][0][1][1]), o5 = cvt2(acc[1][0][1][2], acc[1][0][1][3]);
  const u32 o2 = cvt2(acc[1][1][0][0], acc[1][1][0][1]), o3 = cvt2(acc[1][1][0][2], acc[1][1][0][3]);
  const u32 o6 = cvt2(acc[1][1][1][0], acc[1][1][1][1]);
  bst<0>(v4u{o0, o1, o2, o3}, vo0, rw, so1);
  const u32 o7 = cvt2(acc[1][1][1][2], acc[1][1][1][3]);
  bstp<1024>(v4u{o4, o5, o6, o7}, vo1, rw, so1);
}

template <int W> struct Part {
  static constexpr int NB = (W == 0 || W == 3) ? 2 : 3, E0 = W == 0 ? 0 : 2 * W - 1, NF = W == 0 ? 0 : 1;
  static constexpr DI int lo(int n) { return n == 0 && W > 0 ? 8 - 2 * W : 0; }
  static constexpr DI int hi(int n) { return n == NB - 1 && W < 3 ? 6 - 2 * W : 8; }
  static constexpr DI int tn(int m) { return m < 8 ? NF : W == 0 ? 1 : m - 8 < 2 * W ? 0 : 2; }
  static constexpr DI int tt(int m) { return m < 8 ? m : W == 0 ? m - 8 : m - 8 < 2 * W ? m - 2 * W : m - 8 - 2 * W; }
};

template <int W>
DI void big_gemm(const Params& p, LAS unsigned char* L, int lane, u32 bsoff, u32 psoff) {
  using P = Part<W>;
  constexpr int NB = P::NB;
  const rsrc_t rw = mk(p.ws, WS_SIZE);
  const int r = lane & 15, g = lane >> 4;
  const u32 l0 = (r >> 3) * 1024 + (r & 7) * 128 + ((g ^ (r & 7)) * 16), vo = lane * 16;
  u32 lb[3][2];
#pragma unroll
  for (int i = 0; i < 3; ++i) { lb[i][0] = l0 + i * 65536; lb[i][1] = (l0 ^ 64) + i * 65536; asm volatile("" : "+v"(lb[i][0]), "+v"(lb[i][1])); }
  u32 ctl = L_CTL, pv = 0, fv = 0, bs = bsoff + P::E0 * 1024;
  asm volatile("" : "+v"(ctl));
  u32 have = 0;
  asm volatile("" : "+s"(bs));
  _Pragma("unroll 1") for (int sp = 0; sp < SPIN_L; ++sp) {
    v2u t;
    asm volatile("ds_read_b64 %0, %1 offset:%2\n\ts_waitcnt lgkmcnt(0)" : "=v"(t) : "v"(ctl), "i"(C_FULL) : "memory");
    have = __builtin_amdgcn_readfirstlane(t.x);
    if (have * __builtin_amdgcn_readfirstlane(t.y)) break;
  }
  ST(10);
  v4u B[PD + 1][NB], A[2][8];
  static_for<0, PD>([&](auto S) {
    static_for<0, NB>([&](auto N) { bld<N.value * 1024>(B[S.value][N.value], vo, rw, bs); });
    asm volatile("s_add_u32 %0, %0, 28672" : "+s"(bs)::"scc");
    if constexpr (S.value < 8) dsrd<S.value * 2048>(A[0][S.value], lb[0][0]);
  });
  static_for<PD, 8>([&](auto T) { dsrd<T.value * 2048>(A[0][T.value], lb[0][0]); });
  f32x4 acc[14];
  static_for<0, 32>([&](auto K) {
    constexpr int ks = K.value, k1 = ks + 1, sl = (k1 >> 1) % 9, left = 31 - ks;
    wait_vl<NB*(left < PD - 1 ? left : PD - 1), 4>();
    if constexpr ((ks & 3) == 0) ST(12 + ks / 4);
    static_for<0, 14>([&](auto M) {
      constexpr int m = M.value, ks = K.value;
      if constexpr (m == 4) wait_l<(k1 < 32 ? 4 : 0)>();
      mfma<ks == 0>(acc[m], B[ks % (PD + 1)][P::tn(m)], A[ks & 1][P::tt(m)]);
      if constexpr (m < 8 && k1 < 32) dsrd<(sl & 3) * 16384 + m * 2048>(A[k1 & 1][m], lb[sl >> 2][k1 & 1]);
      if constexpr (!XNOB && m >= 8 && m < 8 + NB && ks + PD < 32) bld<(m - 8) * 1024>(B[(ks + PD) % (PD + 1)][m - 8], vo, rw, bs);
      if constexpr (m == 11 && ks + PD + 1 < 32) asm volatile("s_add_u32 %0, %0, 28672" : "+s"(bs)::"scc");
      constexpr int pr = (ks & 1) && ks <= 13 ? G_RING + ks / 2 : ks == FFNKS ? G_FFN : 0;
      if constexpr (m == 11 && pr) asm volatile("v_mov_b32 %0, %1" : "=v"(pv) : "i"(pr));
      if constexpr (m == 12 && pr) mark<C_PROG + W * 4>(ctl, pv);
      if constexpr (m == 13 && (ks & 3) == 1 && ks < 28) asm volatile("ds_read_b32 %0, %1 offset:%2" : "=v"(fv) : "v"(ctl), "i"(C_FULL));
      if constexpr (m == 12 && (ks & 3) == 2 && ks < 28) { asm volatile("" : "+v"(fv)); have = __builtin_amdgcn_readfirstlane(fv); }
      if constexpr (m == 13 && !(ks & 1) && ks < 30) { if (have < ks / 2 + 2) have = lds_wait_ge(L, L_CTL + C_FULL, ks / 2 + 2); }
    });
  });
  asm volatile("s_nop 7\n\ts_nop 3");
  const u32 qo = (r >> 2) * 28672 + g * 64 + (r & 3) * 16;
  static_for<0, 7>([&](auto I) {
    constexpr int m = 2 * I.value;
    const u32 o0 = cvt2(acc[m][0], acc[m][1]), o1 = cvt2(acc[m][2], acc[m][3]), o2 = cvt2(acc[m + 1][0], acc[m + 1][1]), o3 = cvt2(acc[m + 1][2], acc[m + 1][3]);
    if constexpr (m < 12) bst<(P::E0 + P::tn(m)) * 256>(v4u{o0, o1, o2, o3}, qo, rw, psoff + (P::tt(m) >> 1) * 4 * 28672);
    else bstp<(P::E0 + P::tn(m)) * 256>(v4u{o0, o1, o2, o3}, qo, rw, psoff + (P::tt(m) >> 1) * 4 * 28672);
  });
}

DI void flag_after_drain(rsrc_t rw, u32 vf, u32 ep) {
  asm volatile("s_waitcnt vmcnt(0)\n\tbuffer_store_dword %0, %1, %2, 0 offen sc1" ::"v"(ep), "v"(vf), "s"(rw) : "memory");
}
DI void poll64(const Params& p, u32 off, u32 ep, int lane) {
  const rsrc_t rw = mk(p.ws, WS_SIZE);
  _Pragma("unroll 1") for (int sp = 0; sp < SPIN_G; ++sp) {
    FENCE();
    const u32 v = __builtin_amdgcn_raw_buffer_load_b32(rw, (lane >> 2) * 128 + (lane & 3) * 4, off, SC1);
    if (__all(v == ep)) break;
    __builtin_amdgcn_s_sleep(PSLEEP);
  }
  FENCE();
}
DI void wait_prog(LAS unsigned char* L, u32 v, int lane) { wait_words(L, L_CTL + C_PROG + (lane & 3) * 4, v); }
template <int K0, int K1>
DI void poll_mod(const Params& p, LAS unsigned char* L, int b, u32 ep, int lane) {
  const rsrc_t rw = mk(p.ws, WS_SIZE);
  v4u g[K1 - K0];
  _Pragma("unroll 1") for (int sp = 0; sp < SPIN_G; ++sp) {
    FENCE();
    bool ok = true;
#pragma unroll
    for (int k = K0; k < K1; ++k) { const int ch = min(k * 64 + lane, 671); g[k - K0] = __builtin_amdgcn_raw_buffer_load_b128(rw, ch * 16, WS_MOD + (b & 7) * MODREP, SC1); }
#pragma unroll
    for (int k = K0; k < K1; ++k) ok &= g[k - K0].y == ep && g[k - K0].w == ep;
    if (__all(ok)) break;
  }
#pragma unroll
  for (int k = K0; k < K1; ++k) { const int ch = min(k * 64 + lane, 671); ldsst<v2u>(L, L_MOD + ch * 8, v2u{g[k - K0].x, g[k - K0].z}); }
  FENCE();
}
DI u32 poll_keys(const Params& p, u32 ep, int lane) {
  const rsrc_t rw = mk(p.ws, WS_SIZE);
  u32 best = 0;
  _Pragma("unroll 1") for (int sp = 0; sp < SPIN_G; ++sp) {
    FENCE();
    v2u k[4];
#pragma unroll
    for (int i = 0; i < 4; ++i) k[i] = __builtin_amdgcn_raw_buffer_load_b64(rw, lane * 128, WS_KEY + i * 8192, SC1);
    best = umax(umax(k[0].x, k[1].x), umax(k[2].x, k[3].x));
    if (__all(k[0].y == ep && k[1].y == ep && k[2].y == ep && k[3].y == ep)) break;
  }
  FENCE();
  best = umax(best, dppu<XOR1>(best)); best = umax(best, dppu<XOR2>(best)); best = umax(best, dppu<HMIR>(best)); best = umax(best, dppu<MIR>(best));
  best = max16(best); best = max32(best);
  return __builtin_amdgcn_readfirstlane(best);
}

struct TailIn { v2u xa, xb, wr; };
DI void tail_issue(const Params& p, int b, int w, int lane, TailIn& t) {
  const rsrc_t rx = mk(p.x, 2048 * 896), rr = mk(p.wr, 896);
  const u32 e2 = 128 * w + 8 * (lane >> 2), vx = (lane & 3) * 896 + e2, so = b * 7168;
  asm volatile("buffer_load_dwordx2 %0, %1, %2, %3 offen" : "=v"(t.xa) : "v"(vx), "s"(rx), "s"(so) : "memory");
  asm volatile("buffer_load_dwordx2 %0, %1, %2, %3 offen offset:3584" : "=v"(t.xb) : "v"(vx), "s"(rx), "s"(so) : "memory");
  asm volatile("buffer_load_dwordx2 %0, %1, %2, 0 offen" : "=v"(t.wr) : "v"(e2), "s"(rr) : "memory");
}

typedef u32 v16u __attribute__((ext_vector_type(16)));
typedef u32 v8u __attribute__((ext_vector_type(8)));
template <typename T> DI T* ptr(u32 lo, u32 hi) { return (T*)(((u64)hi << 32) | lo); }

__global__ void __launch_bounds__(512, 2) k_fused(Params) {
  extern __shared__ __attribute__((aligned(16))) unsigned char smem[];
  LAS unsigned char* L = (LAS unsigned char*)smem;
  v16u ka; v8u kb; v2u kc;
  asm volatile("s_load_dwordx8 %1, %3, 0x40\n\ts_load_dwordx16 %0, %3, 0x0\n\ts_load_dwordx2 %2, %3, 0x60" : "=&s"(ka), "=&s"(kb), "=&s"(kc) : "s"(__builtin_amdgcn_kernarg_segment_ptr()));
  SCHED();
  const int b = blockIdx.x, lane = threadIdx.x & 63, w = __builtin_amdgcn_readfirstlane(threadIdx.x >> 6);
  ST0();
  const int xcd = b & 7, j = b >> 3;
  const int half = xcd & 1, ec = j & 3;
  const int sig = (xcd >> 1) * 8 + (j >> 2);
  const int kh = (xcd >> 1) & 1, tt = (xcd >> 2) * 8 + (j >> 2);
  const int cg = (j >> 2) * 32 + xcd * 4 + ec;
  const bool prod = cg < 168;
  const int lw = prod || xcd < 2 ? 56 : 38, rot = XROT ? 2 * ec : 0;
  u32 vt = prod ? lane * 5376 : min((((j >> 2) - (xcd < 2 ? 6 : 5)) * 7 + w) * lw + lane, 783) * 128;
  if (lane >= lw || w >= (prod ? 2 : 7) || !((R1 >> (prod ? 0 : 1)) & 1)) vt = 0x80000000u;
  u32 stc = prod ? (ec * 112 + 56 * (w & 1)) * 5376 + (cg >> 2) * 128 : ec * 100352;
  u32 ntc = prod ? 448 * 5376 : 448 * 896;
  const u32 sw = WSWAP ? (ec & 2) * 16 : 0;
  u32 vm = (64 * w + (lane >> 1)) * 5376 + (lane & 1) * 16, vc = 128 * w + (lane >> 1) * 2, sm = cg * 32 + sw * 5376, sm1 = cg * 32 + (32 - sw) * 5376, sc0 = sw * 2, sc1 = 64 - sw * 2;
  u32 vx = lane < 56 ? lane * 16 : 0x80000000u, sx[8];
#pragma unroll
  for (int i = 0; i < 8; ++i) { sx[i] = (sig * 64 + 8 * w + ((i + rot) & 7)) * 896; asm volatile("" : "+s"(sx[i])); }
  if (w == 7 && lane < 32) ldsst<u32>(L, L_CTL + lane * 4, 0);
  asm volatile("" : "+v"(vm), "+v"(vc), "+v"(vx), "+v"(vt));
  asm volatile("s_waitcnt lgkmcnt(0)" : "+s"(ka), "+s"(kb), "+s"(kc), "+s"(sm), "+s"(sm1), "+s"(sc0), "+s"(sc1), "+s"(stc), "+s"(ntc)::"memory");
  Params p;
  p.x = ptr<const u16>(ka[0], ka[1]); p.sre = ptr<const u16>(ka[2], ka[3]); p.sim = ptr<const u16>(ka[4], ka[5]); p.dre = ptr<const u16>(ka[6], ka[7]);
  p.dim = ptr<const u16>(ka[8], ka[9]); p.wr = ptr<const u16>(ka[10], ka[11]); p.wg = ptr<const u16>(ka[12], ka[13]); p.wu = ptr<const u16>(ka[14], ka[15]);
  p.wd = ptr<const u16>(kb[0], kb[1]); p.c = ptr<const u16>(kb[2], kb[3]); p.wmod = ptr<const u16>(kb[4], kb[5]); p.out = ptr<u16>(kb[6], kb[7]);
  p.ws = ptr<unsigned char>(kc[0], kc[1]);
  const rsrc_t rw = mk(p.ws, WS_SIZE);

  const u16* const wdh = half ? p.dim : p.dre;
  u32 junk = 0;
  touch<0>(junk, vt, mk(prod ? p.wmod : wdh, ntc), stc);
  v4u wm[2]; u32 cv[2];
  if (prod) {
    if (w < 7) {
      const rsrc_t rm = mk(p.wmod, 448 * 2688 * 2), rc = mk(p.c, 896);
      const u32 s0 = sm, s1 = sm1;
      asm volatile("buffer_load_dwordx4 %0, %1, %2, %3 offen" : "=v"(wm[0]) : "v"(vm), "s"(rm), "s"(s0) : "memory");
      asm volatile("buffer_load_dwordx4 %0, %1, %2, %3 offen" : "=v"(wm[1]) : "v"(vm), "s"(rm), "s"(s1) : "memory");
      asm volatile("buffer_load_ushort %0, %1, %2, %3 offen" : "=v"(cv[0]) : "v"(vc), "s"(rc), "s"(sc0) : "memory");
      asm volatile("buffer_load_ushort %0, %1, %2, %3 offen" : "=v"(cv[1]) : "v"(vc), "s"(rc), "s"(sc1) : "memory");
    }
  }
  asm volatile("s_barrier" ::: "memory");
  v4u xr[8];
  x_issue<0, XE>(p, vx, sx, xr);
  u32 ep;
  asm volatile("s_load_dword %0, %1, 0x0" : "=s"(ep) : "s"(p.ws));
  if (prod) {
    if (w < 7) {
      asm volatile("s_waitcnt vmcnt(%4)" : "+v"(wm[0]), "+v"(wm[1]), "+v"(cv[0]), "+v"(cv[1]) : "i"(XE) : "memory");
      ST(0);
      float a[8];
      const u32 h0 = pk(0.f, siluf(lo16(cv[0]))), h1 = pk(0.f, siluf(lo16(cv[1]))), l0 = h0 >> 16, l1 = h1 >> 16;
#pragma unroll
      for (int q = 0; q < 4; ++q) { a[2 * q] = dot2(wm[1][q], l1, dot2(wm[0][q], l0, 0.f)); a[2 * q + 1] = dot2(wm[1][q], h1, dot2(wm[0][q], h0, 0.f)); }
      float v0 = h16(h32(a[0], a[4]), h32(a[2], a[6])), v1 = h16(h32(a[1], a[5]), h32(a[3], a[7]));
      v0 += dppf<ROR4>(v0); v1 += dppf<ROR4>(v1); v0 += dppf<ROR8>(v0); v1 += dppf<ROR8>(v1); v0 += dppf<XOR2>(v0); v1 += dppf<XOR2>(v1);
      if ((lane & 14) == 0) ldsst<f32x2>(L, L_RED + w * 64 + (lane & 1) * 32 + (lane >> 5) * 16 + ((lane >> 4) & 1) * 8, f32x2{v0, v1});
    } else {
      ST(0);
    }
    BAR();
  } else {
    ST(0);
  }
  asm volatile("s_waitcnt lgkmcnt(0)" : "+s"(ep)::"memory");
  ep += 1;
  if (prod && w == 7) {
    f32x2 t = ldsld<f32x2>(L, L_RED + (lane & 7) * 8);
#pragma unroll
    for (int k = 1; k < 7; ++k) t += ldsld<f32x2>(L, L_RED + k * 64 + (lane & 7) * 8);
    u32 v = pk(t[0], t[1]);
    const int part = cg / 28;
    if (part == 1 || part == 4) v = pk(1.f + lo16(v), 1.f + hi16(v));
    __builtin_amdgcn_raw_buffer_store_b64(v2u{v, ep}, rw, (lane >> 3) * MODREP + (lane & 7) * 8, WS_MOD + cg * 64, SC1);
  }
  if (prod) asm volatile("s_barrier" ::: "memory");
  ST(1);
  x_issue<XE, 8>(p, vx, sx, xr);
  ST(2);
  f32x2 n[8][4];
  {
    const rsrc_t rd = mk(wdh, 448 * 896);
    const u32 r7 = lane >> 3, vo = r7 * 896 + (((lane & 7) ^ r7) * 16), d0 = w * 1024;
    const u32 pi = 8 * (lane & 15) + w;
    u32 tv = ec * 112 * 896 + pi * 128 + ((pi * 9363) >> 16) * 6272;
    u32 so = __builtin_amdgcn_readfirstlane(tv);
    asm volatile("" : "+v"(tv));
    asm volatile("" : "+s"(so));
    auto piece = [&](auto H) {
      const u32 v_ = vo, d_ = d0, t_ = tv, s_ = so; const rsrc_t r_ = rd; u32 nx;
      asm volatile("s_add_u32 m0, %4, %6\n\tv_readlane_b32 %0, %5, %7\n\tbuffer_load_dwordx4 %1, %2, %3 offen lds"
                   : "=&s"(nx) : "v"(v_), "s"(r_), "s"(s_), "s"(d_), "v"(t_), "i"(H.value * 8192), "i"(H.value + 1) : "memory", "scc");
      so = nx;
    };
    static_for<0, NP0>([&](auto H) { piece(H); asm volatile("s_nop 1"); });
    ST(27);
    ln_stats(xr, lane, n, piece);
    if (w < 2) piece(IC<12>{});
    ST(3);
  }
  HAddr ha = h_addr(lane, w, rot);
  TailIn ti;
  HMod hm;

  if (w == 7) {
    u32 tsink = 0;
    const rsrc_t ra = mk(half ? p.sim : p.sre, 2048 * 4096);
    const u32 va = (lane & 31) * 4096 + (lane >> 5) * 128, sa = (tt * 128 + ec * 32) * 4096 + kh * 2048;
    static_for<0, TA>([&](auto I) { u32 k_ = tsink; touch<I.value * 256>(k_, va, ra, sa); tsink = k_; });
    asm volatile("s_waitcnt vmcnt(%1)" : "+v"(junk) : "i"(TA) : "memory");
    mark<C_WD + 28>(L_CTL, 1);
    const unsigned char* tp = lane < 56 ? (const unsigned char*)p.x + (b * 7168 + lane * 128) : (const unsigned char*)p.wr + (lane & 7) * 112;
    asm volatile("" : "+v"(tp));
    poll_mod<0, 4>(p, L, b, ep, lane);
    static_for<TA, TA + TB>([&](auto I) { u32 k_ = tsink; touch<I.value * 256>(k_, va, ra, sa); tsink = k_; });
    asm volatile("global_load_dword %0, %1, off" : "+v"(tsink) : "v"(tp) : "memory");
    ST(4);
    BAR();
    h_prep(hm, ha);
    BAR();
    __builtin_amdgcn_s_setprio(HPRIO);
    h_write(n, lane, ha, hm);
    mark<C_H2 + 12>(L_CTL, 1);
    ST(6);
    poll_mod<4, 11>(p, L, b, ep, lane);
    asm volatile("s_waitcnt vmcnt(0)" : "+v"(tsink)::"memory");
    ST(7);
    wait_prog(L, G_H, lane);
    poll64(p, WS_F1 + ((half * 4 + ec) * 32 + kh * 16) * 128, ep, lane);
    mark<C_X1>(L_CTL, 1);
    ST(8);
    {
      const u32 vg = lane < 56 ? (lane >> 2) * 3584 + (lane & 3) * 128 : 0x80000000u, sg = j * 14 * 3584 + ((448 * xcd) & ~127);
      const u32 vd = lane < 49 ? lane * 128 : 0x80000000u, sd = xcd * 224 * 896 + j * 6272;
      wait_prog(L, G_RING + FTS, lane);
      static_for<TA + TB, 8>([&](auto I) { u32 k_ = tsink; touch<I.value * 256>(k_, va, ra, sa); tsink = k_; });
      touch<0>(tsink, vg, mk(p.wg, 448 * 3584), sg); touch<0>(tsink, vg, mk(p.wu, 448 * 3584), sg); touch<0>(tsink, vd, mk(p.wd, 1792 * 896), sd);
      asm volatile("s_waitcnt vmcnt(0)" : "+v"(tsink)::"memory");
    }
    ST(9);
    if (b < NFFN) {
      const int fg = (b & 7) * 14 + (b >> 3);
      wait_prog(L, XNOF ? G_X2 : G_FFN, lane);
      ST(10);
      u32 wgp[7][8], wup[7][8], wdp[8][8];
      {
        const rsrc_t rg = mk(p.wg, 448 * 3584), ru = mk(p.wu, 448 * 3584), rd = mk(p.wd, 1792 * 896);
        v4u tg[7][2], tu[7][2], td[8][2];
        const u32 vo = (lane >> 1) * 4 * 3584 + (lane & 1) * 16, vo6 = (lane >> 1) * 2 * 3584 + (lane & 1) * 16;
#pragma unroll
        for (int t = 0; t < 7; ++t)
#pragma unroll
          for (int k = 0; k < 2; ++k) {
            const u32 so = fg * 32 + (t < 6 ? 128 * (t >> 1) + 2 * (t & 1) + k : 384 + k) * 3584;
            tg[t][k] = __builtin_amdgcn_raw_buffer_load_b128(rg, t < 6 ? vo : vo6, so, AUX_FFN);
            tu[t][k] = __builtin_amdgcn_raw_buffer_load_b128(ru, t < 6 ? vo : vo6, so, AUX_FFN);
          }
        const u32 vd = lane < 56 ? lane * 16 : 0x80000000u;
#pragma unroll
        for (int t = 0; t < 8; ++t)
#pragma unroll
          for (int k = 0; k < 2; ++k) td[t][k] = __builtin_amdgcn_raw_buffer_load_b128(rd, vd, (fg * 16 + 2 * t + k) * 896, AUX_FFN);
        SCHED();
#pragma unroll
        for (int t = 0; t < 7; ++t)
#pragma unroll
          for (int q = 0; q < 4; ++q) {
            wgp[t][2 * q] = __builtin_amdgcn_perm(tg[t][1][q], tg[t][0][q], 0x05040100); wgp[t][2 * q + 1] = __builtin_amdgcn_perm(tg[t][1][q], tg[t][0][q], 0x07060302);
            wup[t][2 * q] = __builtin_amdgcn_perm(tu[t][1][q], tu[t][0][q], 0x05040100); wup[t][2 * q + 1] = __builtin_amdgcn_perm(tu[t][1][q], tu[t][0][q], 0x07060302);
            asm volatile("" : "+v"(wgp[t][2 * q]), "+v"(wgp[t][2 * q + 1]), "+v"(wup[t][2 * q]), "+v"(wup[t][2 * q + 1]));
          }
#pragma unroll
        for (int t = 0; t < 8; ++t)
#pragma unroll
          for (int q = 0; q < 4; ++q) {
            wdp[t][2 * q] = __builtin_amdgcn_perm(td[t][1][q], td[t][0][q], 0x05040100); wdp[t][2 * q + 1] = __builtin_amdgcn_perm(td[t][1][q], td[t][0][q], 0x07060302);
            asm volatile("" : "+v"(wdp[t][2 * q]), "+v"(wdp[t][2 * q + 1]));
          }
      }
      ST(11);
      wait_prog(L, G_X2, lane);
      poll64(p, WS_F2 + (b >> 4) * 16 * 128, ep, lane);
      ST(12);
      BAR();
      BAR();
      BAR();
      ST(13);
      const u32 key = poll_keys(p, ep, lane);
      ST(14);
      const u32 cb = WS_CAND + ((2047 - (key & 0x7ff)) >> 3) * 3584;
      u32 hp[7];
      _Pragma("unroll 1") for (int sp = 0; sp < SPIN_G; ++sp) {
        FENCE();
        v4u q[3];
#pragma unroll
        for (int i = 0; i < 3; ++i) q[i] = __builtin_amdgcn_raw_buffer_load_b128(rw, (lane >> 1) * 16 + i * 512, cb, SC1);
        const v2u q6 = __builtin_amdgcn_raw_buffer_load_b64(rw, (lane >> 1) * 8 + 1536, cb, SC1);
        bool ok = q6.y == ep;
#pragma unroll
        for (int i = 0; i < 3; ++i) { ok &= q[i].y == ep && q[i].w == ep; hp[2 * i] = q[i].x; hp[2 * i + 1] = q[i].z; }
        hp[6] = q6.x;
        if (__all(ok)) break;
      }
      FENCE();
      ST(15);
      float g[8], u[8];
#pragma unroll
      for (int n = 0; n < 8; ++n) { g[n] = 0.f; u[n] = 0.f; }
#pragma unroll
      for (int t = 0; t < 7; ++t)
#pragma unroll
        for (int n = 0; n < 8; ++n) { g[n] = dot2(wgp[t][n], hp[t], g[n]); u[n] = dot2(wup[t][n], hp[t], u[n]); }
      const float g0 = h16(h32(g[0], g[4]), h32(g[2], g[6])), g1 = h16(h32(g[1], g[5]), h32(g[3], g[7]));
      const float u0 = h16(h32(u[0], u[4]), h32(u[2], u[6])), u1 = h16(h32(u[1], u[5]), h32(u[3], u[7]));
      const bool b2 = lane & 4;
      const float gr1 = dppf<SHR4>(g1), gl0 = dppf<SHL4>(g0), ur1 = dppf<SHR4>(u1), ul0 = dppf<SHL4>(u0);
      float gg = b2 ? g1 + gr1 : g0 + gl0;
      float uu = b2 ? u1 + ur1 : u0 + ul0;
      gg += dppf<ROR8>(gg); uu += dppf<ROR8>(uu); gg += dppf<XOR2>(gg); uu += dppf<XOR2>(uu);
      const float act = rbf(siluf(rbf(gg))) * rbf(uu);
      const u32 ap = pk(act, dppf<SHL4>(act));
      float o[8];
#pragma unroll
      for (int n = 0; n < 8; ++n) o[n] = 0.f;
#pragma unroll
      for (int t = 0; t < 8; ++t) {
        const u32 s = __builtin_amdgcn_readlane(ap, (t >> 2) + ((t >> 1) & 1) * 32 + (t & 1) * 16);
#pragma unroll
        for (int n = 0; n < 8; ++n) o[n] = dot2(wdp[t][n], s, o[n]);
      }
      if (lane < 56) {
#pragma unroll
        for (int s = 0; s < 2; ++s) {
          const v4u gr = {pk(o[4 * s], o[4 * s + 1]), ep, pk(o[4 * s + 2], o[4 * s + 3]), ep};
          bstp<0>(gr, (lane >> 2) * 2 * 7168 + (lane & 3) * 16, rw, WS_FP + s * 7168 + fg * 64);
        }
      }
      ST(16);
      STEND(17);
    } else {
      wait_prog(L, G_X2, lane);
      poll64(p, WS_F2 + (b >> 4) * 16 * 128, ep, lane);
      ST(10);
      BAR();
      BAR();
      BAR();
      if (b < NFFN + NFIN) {
        const int f = b - NFFN, col = 32 * (f >> 1) + 8 * (lane & 3) + 4 * (f & 1);
        const v2u gb = ldsld<v2u>(L, L_MOD + 5 * 896 + col * 2);
        ST(13);
        const u32 key = poll_keys(p, ep, lane);
        ST(20);
        const u32 row = 2047 - (key & 0x7ff), kb = (key & ~0x7ffu) | 0x400u;
        const float rv = __uint_as_float((kb & 0x80000000u) ? (kb & 0x7fffffffu) : ~kb);
        const float sg = rbf(__builtin_amdgcn_rcpf(1.f + __expf(-rbf(rv))));
        v4u z;
        _Pragma("unroll 1") for (int sp = 0; sp < SPIN_G; ++sp) {
          FENCE();
          z = __builtin_amdgcn_raw_buffer_load_b128(rw, col * 4, WS_CAND + (row >> 3) * 3584 + 1792, SC1);
          const u32 dn = __builtin_amdgcn_raw_buffer_load_b32(rw, 0, WS_DONE + (row >> 3) * 128, SC1);
          if (__all(z.y == ep && z.w == ep && dn == ep)) break;
        }
        ST(21);
        float s0, s1, s2, s3;
        _Pragma("unroll 1") for (int sp = 0; sp < SPIN_G; ++sp) {
          FENCE();
          v4u q[7];
#pragma unroll
          for (int i = 0; i < 7; ++i) q[i] = __builtin_amdgcn_raw_buffer_load_b128(rw, lane * 16, WS_FP + f * 7168 + i * 1024, SC1);
          bool ok = true; s0 = s1 = s2 = s3 = 0.f;
#pragma unroll
          for (int i = 0; i < 7; ++i) { ok &= q[i].y == ep && q[i].w == ep; s0 += lo16(q[i].x); s1 += hi16(q[i].x); s2 += lo16(q[i].z); s3 += hi16(q[i].z); }
          if (__all(ok)) break;
        }
        FENCE();
        ST(22);
        float a0 = h32(s0, s2), a1 = h32(s1, s3);
        a0 = h16(a0, a0); a1 = h16(a1, a1);
        a0 += dppf<ROR4>(a0); a1 += dppf<ROR4>(a1); a0 += dppf<ROR8>(a0); a1 += dppf<ROR8>(a1);
        const u32 zz = lane < 32 ? z.x : z.z, gg = lane < 32 ? gb.x : gb.y;
        const u32 o = pk(lo16(zz) + lo16(gg) * (rbf(a0) * sg), hi16(zz) + hi16(gg) * (rbf(a1) * sg));
        if ((lane & 28) == 0) __builtin_amdgcn_raw_buffer_store_b32(o, mk(p.out, 2048 * 896), col * 2 + (lane >> 5) * 4, row * 896, SC1);
        if (f == 0 && lane == 0) __builtin_amdgcn_raw_buffer_store_b32(ep, rw, 0, WS_EPOCH, SC1);
        ST(23);
        STEND(24);
      } else {
        STEND(13);
      }
    }
#pragma unroll
    for (int i = 0; i < 8; ++i) asm volatile("" : "=v"(n[i][0]), "=v"(n[i][1]), "=v"(n[i][2]), "=v"(n[i][3]));
    asm volatile("s_endpgm" : "=v"(junk), "=v"(ha.a[0]), "=v"(ha.a[1]), "=v"(ha.a[2]), "=v"(ha.a[3]), "=v"(ha.a[4]), "=v"(ha.a[5]), "=v"(ha.a[6]), "=v"(ha.a[7]), "=v"(ha.m)::"memory");
  }
  asm volatile("s_waitcnt vmcnt(0)" : "+v"(junk)::"memory");
  mark<C_WD>(L_CTL + w * 4, 1);
  if (w < 4) {
    wait_words(L, L_CTL + C_WD + (lane & 7) * 4, 1);
    ST(4);
    v4u wf[2][14];
    {
      const int r = lane & 15, g = lane >> 4;
      const u32 l0 = w * 28672 + (r >> 3) * 7168 + (r & 7) * 128 + ((g ^ (r & 7)) * 16), l1 = l0 ^ 64;
#pragma unroll
      for (int ks = 0; ks < 14; ++ks) wf[0][ks] = ldsld<v4u>(L, ((ks & 1) ? l1 : l0) + (ks >> 1) * 1024);
      const u32 d = w < 3 ? 14336 : 0;
#pragma unroll
      for (int ks = 0; ks < 14; ++ks) wf[1][ks] = ldsld<v4u>(L, ((ks & 1) ? l1 : l0) + d + (ks >> 1) * 1024);
#pragma unroll
      for (int ks = 0; ks < 14; ++ks) asm volatile("" : "+v"(wf[0][ks]), "+v"(wf[1][ks]));
    }
    const u32 oob = 0x80000000u;
    u32 pw = L_CTL + w * 4;
    asm volatile("" : "+v"(pw));
    u32 l0, l1, vo0 = lane * 16, vo1 = w < 3 ? vo0 : oob, so0 = WS_AB + ((half * 64 + sig * 2) * 28 + ec * 7 + 2 * w) * 1024, so1 = so0 + 28 * 1024;
    u32 vf1 = lane == 0 ? WS_F1 + ((half * 4 + ec) * 32 + sig) * 128 + w * 4 : oob, vf2 = lane == 0 ? WS_F2 + (tt * 16 + (half * 2 + kh) * 4 + ec) * 128 + w * 4 : oob, vep = ep;
    { const int r = lane & 15, g = lane >> 4; l0 = L_H + (r >> 3) * 7168 + (r & 7) * 128 + ((g ^ (r & 7)) * 16); l1 = l0 ^ 64; }
    u32 hw = L_CTL + C_H2 + (lane & 3) * 4;
    asm volatile("" : "+v"(l0), "+v"(l1), "+v"(vo0), "+v"(vo1), "+v"(vf1), "+v"(vf2), "+v"(vep), "+v"(hw));
    asm volatile("" : "+s"(so0), "+s"(so1));
    ST(5);
    BAR();
    ST(6);
    h_prep(hm, ha);
    h_write(n, lane, ha, hm);
    BAR();
    ST(7);
    small_gemm(L, rw, wf, l0, l1, vo0, vo1, so0, so1, hw);
    marki<C_PROG, G_H>(pw);
    ST(8);
    flag_after_drain(rw, vf1, vep);
    if (W3CTL == G_X1) mark<C_PROG>(pw, G_X1);
    tail_issue(p, b, w, lane, ti);
    ST(9);
    const u32 bs = WS_AB + ((half * 64 + kh * 32) * 28 + ec * 7) * 1024;
    const u32 ps = WS_PART + tt * 16 * 28672 + (half * 2 + kh) * 7168 + ec * 7 * 256;
    if (w == 0) big_gemm<0>(p, L, lane, bs, ps);
    else if (w == 1) big_gemm<1>(p, L, lane, bs, ps);
    else if (w == 2) big_gemm<2>(p, L, lane, bs, ps);
    else big_gemm<3>(p, L, lane, bs, ps);
    ST(26);
    flag_after_drain(rw, vf2, vep);
    mark<C_PROG>(pw, G_X2);
    ST(11);
  } else {
    ST(4);
    BAR();
    h_prep(hm, ha);
    BAR();
    __builtin_amdgcn_s_setprio(HPRIO);
    h_write(n, lane, ha, hm);
    mark<C_H2 - 16>(L_CTL + w * 4, 1);
    ST(6);
    if (w == 4) {
      const rsrc_t ra = mk(half ? p.sim : p.sre, 2048 * 4096);
      const u32 r7 = lane >> 3, sa0 = tt * 128 * 4096 + kh * 2048, sa9 = sa0 + 9 * 128;
      u32 va[16];
#pragma unroll
      for (int pi = 0; pi < 16; ++pi) {
        va[pi] = (8 * (r7 >> 2) + (r7 & 3) + 32 * (pi >> 2) + 16 * (pi & 1) + 4 * ((pi >> 1) & 1)) * 4096 + (((lane & 7) ^ r7) * 16);
        asm volatile("" : "+v"(va[pi]));
      }
      auto slices = [&](auto K0, auto K1) {
        static_for<K0.value * 16, K1.value * 16>([&](auto I) { constexpr int k = I.value >> 4, pi = I.value & 15; dmac<(k % 9) * 16384 + pi * 1024, (k % 9) * 128>(va[pi], ra, k < 9 ? sa0 : sa9); });
      };
      slices(IC<0>{}, IC<NPRE>{});
      ST(7);
      wait_prog(L, W3CTL, lane);
      ST(8);
      static_for<NPRE, 9>([&](auto K) {
        slices(K, IC<K.value + 1>{});
        if constexpr (K.value >= LAG) { wait_v<16 * LAG>(); mark<C_FULL>(L_CTL, K.value + 1 - LAG); }
      });
      ST(9);
      VM0();
      mark<C_FULL>(L_CTL, 9);
      ST(10);
      static_for<9, 16>([&](auto K) {
        constexpr int k = K.value;
        wait_prog(L, G_RING + k - 9, lane);
        if (!XNOA) slices(K, IC<k + 1>{});
        if constexpr (k > 9) { wait_v<16>(); mark<C_FULL>(L_CTL, k); }
        if constexpr (k & 1) ST(11 + (k - 9) / 2);
      });
      VM0();
      mark<C_FULL>(L_CTL, 16);
    } else {
      wait_words(L, L_CTL + C_X1, 1);
    }
    tail_issue(p, b, w, lane, ti);
    asm volatile("s_waitcnt vmcnt(0)" : "+v"(ti.xa), "+v"(ti.xb), "+v"(ti.wr)::"memory");
    ST(15);
  }

  {
    const int ci = lane & 3, e0 = 64 * w + 4 * (lane >> 2);
    const v2u xa = ti.xa, xb = ti.xb, wr = ti.wr;
    const v2u ga = ldsld<v2u>(L, L_MOD + 2 * 896 + e0 * 2), sb = ldsld<v2u>(L, L_MOD + 3 * 896 + e0 * 2), cb = ldsld<v2u>(L, L_MOD + 4 * 896 + e0 * 2), gb = ldsld<v2u>(L, L_MOD + 5 * 896 + e0 * 2);
    float gaf[4] = {lo16(ga.x), hi16(ga.x), lo16(ga.y), hi16(ga.y)}, sbf[4] = {lo16(sb.x), hi16(sb.x), lo16(sb.y), hi16(sb.y)};
    float cbf[4] = {lo16(cb.x), hi16(cb.x), lo16(cb.y), hi16(cb.y)}, gbf[4] = {lo16(gb.x), hi16(gb.x), lo16(gb.y), hi16(gb.y)};
    float wrf[4] = {lo16(wr.x), hi16(wr.x), lo16(wr.y), hi16(wr.y)};
    float va[4] = {lo16(xa.x), hi16(xa.x), lo16(xa.y), hi16(xa.y)}, vb[4] = {lo16(xb.x), hi16(xb.x), lo16(xb.y), hi16(xb.y)};
    float wp[4], Wp = 0.f, Cp = 0.f;
#pragma unroll
    for (int i = 0; i < 4; ++i) { wp[i] = cbf[i] * wrf[i]; Wp += wp[i]; Cp += sbf[i] * wrf[i]; }
#pragma unroll
    for (int i = 0; i < 4; ++i) asm volatile("" : "+v"(wp[i]), "+v"(va[i]), "+v"(vb[i]), "+v"(gaf[i]));
    asm volatile("" : "+v"(Wp), "+v"(Cp));
    ST(20);
    BAR();
    ST(21);
    v4u q[4];
#pragma unroll
    for (int s = 0; s < 4; ++s) q[s] = __builtin_amdgcn_raw_buffer_load_b128(rw, lane * 16, WS_PART + b * 28672 + s * 7168 + w * 1024, SC1);
#pragma unroll
    for (int i = 0; i < 2; ++i) {
      va[2 * i] += gaf[2 * i] * ((lo16(q[0][i]) + lo16(q[1][i])) - (lo16(q[2][i]) + lo16(q[3][i])));
      va[2 * i + 1] += gaf[2 * i + 1] * ((hi16(q[0][i]) + hi16(q[1][i])) - (hi16(q[2][i]) + hi16(q[3][i])));
      vb[2 * i] += gaf[2 * i] * ((lo16(q[0][2 + i]) + lo16(q[1][2 + i])) - (lo16(q[2][2 + i]) + lo16(q[3][2 + i])));
      vb[2 * i + 1] += gaf[2 * i + 1] * ((hi16(q[0][2 + i]) + hi16(q[1][2 + i])) - (hi16(q[2][2 + i]) + hi16(q[3][2 + i])));
    }
    ST(22);
    float s1a = (va[0] + va[1]) + (va[2] + va[3]), s1b = (vb[0] + vb[1]) + (vb[2] + vb[3]);
    float s2a = (va[0] * va[0] + va[1] * va[1]) + (va[2] * va[2] + va[3] * va[3]), s2b = (vb[0] * vb[0] + vb[1] * vb[1]) + (vb[2] * vb[2] + vb[3] * vb[3]);
    float s3a = (va[0] * wp[0] + va[1] * wp[1]) + (va[2] * wp[2] + va[3] * wp[3]), s3b = (vb[0] * wp[0] + vb[1] * wp[1]) + (vb[2] * wp[2] + vb[3] * wp[3]);
    float v0 = h16(h32(s1a, s1b), h32(s2a, s2b)), v1 = h16(h32(s3a, s3b), h32(Wp, Cp));
    v0 += dppf<ROR4>(v0); v1 += dppf<ROR4>(v1); v0 += dppf<ROR8>(v0); v1 += dppf<ROR8>(v1);
    if ((lane & 12) == 0) {
      const u32 o = L_ST + ((lane >> 4) & 1) * 256 + w * 32 + (lane >> 5) * 16 + ci * 4;
      ldsst<float>(L, o, v0); ldsst<float>(L, o + 512, v1);
    }
    BAR();
    float t[4];
#pragma unroll
    for (int k = 0; k < 4; ++k) { t[k] = ldsld<float>(L, L_ST + k * 256 + lane * 4); t[k] = lane < 56 ? t[k] : 0.f; }
#pragma unroll
    for (int k = 0; k < 4; ++k) { t[k] += dppf<ROR8>(t[k]); t[k] = h16(t[k], t[k]); t[k] = h32(t[k], t[k]); }
    const bool up = lane & 4;
    const float oth = dppf<ROR4>(t[3]), Wt = up ? oth : t[3], Ct = up ? t[3] : oth;
    const float mu = t[0] * (1.f / 448.f);
    const float rs = __builtin_amdgcn_rsqf(fmaxf(t[1] * (1.f / 448.f) - mu * mu, 0.f) + 1e-5f);
    const float rr = rs * (t[2] - mu * Wt) + Ct;
    u32 key = __float_as_uint(rr);
    key = (key & 0x80000000u) ? ~key : (key | 0x80000000u);
    key = (key & ~0x7ffu) | (2047 - (b * 8 + (lane & 7)));
    key = umax(key, dppu<XOR1>(key)); key = umax(key, dppu<XOR2>(key)); key = umax(key, dppu<HMIR>(key));
    if (w == 0 && lane == 0) __builtin_amdgcn_raw_buffer_store_b64(v2u{key, ep}, rw, 0, WS_KEY + b * 128, SC1);
    ST(23);
    const float mo = dppf<ROR4>(mu), ro = dppf<ROR4>(rs);
    const float mua = up ? mo : mu, mub = up ? mu : mo, rsa = up ? ro : rs, rsb = up ? rs : ro;
    float ha[4], hb[4];
#pragma unroll
    for (int i = 0; i < 4; ++i) { ha[i] = (va[i] - mua) * rsa * cbf[i] + sbf[i]; hb[i] = (vb[i] - mub) * rsb * cbf[i] + sbf[i]; }
    ldsst<v2u>(L, L_H2 + ci * 896 + e0 * 2, v2u{pk(ha[0], ha[1]), pk(ha[2], ha[3])});
    ldsst<v2u>(L, L_H2 + (ci + 4) * 896 + e0 * 2, v2u{pk(hb[0], hb[1]), pk(hb[2], hb[3])});
    ldsst<v2u>(L, L_Z + ci * 896 + e0 * 2, v2u{pk(va[0] + gbf[0] * ha[0], va[1] + gbf[1] * ha[1]), pk(va[2] + gbf[2] * ha[2], va[3] + gbf[3] * ha[3])});
    ldsst<v2u>(L, L_Z + (ci + 4) * 896 + e0 * 2, v2u{pk(vb[0] + gbf[0] * hb[0], vb[1] + gbf[1] * hb[1]), pk(vb[2] + gbf[2] * hb[2], vb[3] + gbf[3] * hb[3])});
    BAR();
    if (w == 0) {
      const rsrc_t ro_ = mk(p.out, 2048 * 896);
      v4u z[7];
#pragma unroll
      for (int i = 0; i < 7; ++i) z[i] = ldsld<v4u>(L, L_Z + i * 1024 + lane * 16);
      static_for<0, 7>([&](auto I) { bstp<(I.value & 3) * 1024>(z[I.value], lane * 16, ro_, b * 7168 + (I.value >> 2) * 4096); });
      VM0();
      if (lane == 0) __builtin_amdgcn_raw_buffer_store_b32(ep, rw, 0, WS_DONE + b * 128, SC1);
    } else if (w < 3) {
      const u32 best = (2047 - (__builtin_amdgcn_readfirstlane(key) & 0x7ff)) & 7;
      const u32 src = (w == 1 ? L_H2 : L_Z) + best * 896;
#pragma unroll
      for (int k = 0; k < 2; ++k) {
        if (k * 64 + lane < 112) {
          const v2u d = ldsld<v2u>(L, src + (k * 64 + lane) * 8);
          bstp<0>(v4u{d.x, ep, d.y, ep}, lane * 16 + k * 1024, rw, WS_CAND + b * 3584 + (w - 1) * 1792);
        }
      }
    }
    ST(24);
    STEND(25);
  }
}

extern "C" void kernel_launch(void* const* d_in, const int* in_sizes, int n_in, void* d_out, int out_size, void* d_ws, size_t ws_size, hipStream_t stream) {
  Params p;
  p.x = (const u16*)d_in[0]; p.sre = (const u16*)d_in[1]; p.sim = (const u16*)d_in[2]; p.dre = (const u16*)d_in[3]; p.dim = (const u16*)d_in[4];
  p.wr = (const u16*)d_in[6]; p.wg = (const u16*)d_in[7]; p.wu = (const u16*)d_in[8]; p.wd = (const u16*)d_in[9]; p.c = (const u16*)d_in[10]; p.wmod = (const u16*)d_in[11];
  p.out = (u16*)d_out; p.ws = (unsigned char*)d_ws;
  (void)hipFuncSetAttribute((const void*)k_fused, hipFuncAttributeMaxDynamicSharedMemorySize, L_TOTAL);
  k_fused<<<256, 512, L_TOTAL, stream>>>(p);
}
```

```cpp
#include <hip/hip_runtime.h>
#include <stdint.h>

typedef unsigned short u16;
typedef unsigned u32;
typedef unsigned long long u64;
typedef u32 v4u __attribute__((ext_vector_type(4)));
typedef u32 v2u __attribute__((ext_vector_type(2)));
typedef float f32x4 __attribute__((ext_vector_type(4)));
typedef float f32x2 __attribute__((ext_vector_type(2)));
typedef __bf16 bf16x2 __attribute__((ext_vector_type(2)));
typedef __amdgpu_buffer_rsrc_t rsrc_t;
#define LAS __attribute__((address_space(3)))
#define DI __device__ __forceinline__

#define AUX_FFN 0
#define PD 5
#define PSLEEP 1
#define TB 5
#define LAG 1
#define NPRE 0
#define FFNKS 15
#define SC1 16
#define PW 7
#define XST 1
#define KPL 2
#define FD 120
#define KSL 0
#define PDLY 16
#define XPT 1
#define TL 1
#define TLX 1
#define WD 9
#define WE 0
#define PGS 3
#define CPW 0
#define PD1 8
#define PWIN 4
#define TLO 1

static constexpr u32 WS_EPOCH = 0;
static constexpr u32 WS_MOD = 4096, MODREP = 10880;
static constexpr u32 WS_F1 = 98304, WS_F2 = 131072, WS_DONE = 196608;
static constexpr u32 WS_CAND = 262144;
static constexpr u32 WS_FP = 1310720;
static constexpr u32 WS_AB = 2097152;
static constexpr u32 WS_PART = 6291456;
static constexpr u32 WS_KEYR = 14155776;
static constexpr u32 WS_SIZE = 16u << 20;
static constexpr u32 L_H = 100352;
static constexpr u32 L_MOD = 157696;
static constexpr u32 L_CTL = L_MOD + 5376;
static constexpr u32 L_RED = L_CTL + 256;
static constexpr u32 L_TOTAL = 163840;
static constexpr u32 C_PROG = 0, C_FULL = 16, C_X1 = 20, C_H = 32, C_M = 64, C_S = 80, C_CS = 84, C_XL = 96, C_WL = 128, C_N1 = 160, C_N2 = 164;
static constexpr u32 G_H = 1, G_X1 = 2, G_RING = 3, G_FFN = 10, G_X2 = 11;
static constexpr u32 L_ST = 0;
static constexpr u32 EP_STEP = 1;
static constexpr int SPIN_G = 1 << 14, SPIN_L = 1 << 18;
static constexpr int NFFN = 112, NFIN = 28;

struct Params {
  const u16 *x, *sre, *sim, *dre, *dim, *wr, *wg, *wu, *wd, *c, *wmod;
  u16* out;
  unsigned char* ws;
};

template <int V> struct IC { static constexpr int value = V; };
template <int A, int B, typename F> DI void static_for(F f) { if constexpr (A < B) { f(IC<A>{}); static_for<A + 1, B>(f); } }
DI rsrc_t mk(const void* p, u32 n) { return __builtin_amdgcn_make_buffer_rsrc((void*)p, 0, n, 0x00020000); }
DI float lo16(u32 v) { return __uint_as_float(v << 16); }
DI float hi16(u32 v) { return __uint_as_float(v & 0xffff0000u); }
DI u32 pk(float a, float b) { f32x2 v = {a, b}; return __builtin_bit_cast(u32, __builtin_convertvector(v, bf16x2)); }
DI float rbf(float a) { return lo16(pk(a, a)); }
DI float dot2(u32 a, u32 b, float c) { return __builtin_amdgcn_fdot2_f32_bf16(__builtin_bit_cast(bf16x2, a), __builtin_bit_cast(bf16x2, b), c, false); }
template <int CTRL> DI float dppf(float v) { return __uint_as_float(__builtin_amdgcn_update_dpp(0, __float_as_uint(v), CTRL, 0xf, 0xf, true)); }
template <int CTRL> DI u32 dppu(u32 v) { return __builtin_amdgcn_update_dpp(0, v, CTRL, 0xf, 0xf, true); }
static constexpr int XOR1 = 0xB1, XOR2 = 0x4E, HMIR = 0x141, MIR = 0x140, ROR4 = 0x124, ROR8 = 0x128, SHL4 = 0x104, SHR4 = 0x114;
DI float h32(float a, float b) { auto r = __builtin_amdgcn_permlane32_swap(__float_as_uint(a), __float_as_uint(b), false, false); return __uint_as_float(r[0]) + __uint_as_float(r[1]); }
DI float h16(float a, float b) { auto r = __builtin_amdgcn_permlane16_swap(__float_as_uint(a), __float_as_uint(b), false, false); return __uint_as_float(r[0]) + __uint_as_float(r[1]); }
DI u32 max32(u32 a) { auto r = __builtin_amdgcn_permlane32_swap(a, a, false, false); return r[0] > r[1] ? r[0] : r[1]; }
DI u32 max16(u32 a) { auto r = __builtin_amdgcn_permlane16_swap(a, a, false, false); return r[0] > r[1] ? r[0] : r[1]; }
DI u32 umax(u32 a, u32 b) { return a > b ? a : b; }
DI float rdl(float v, int l) { return __uint_as_float(__builtin_amdgcn_readlane(__float_as_uint(v), l)); }
DI float siluf(float v) { return v * __builtin_amdgcn_rcpf(1.f + __expf(-v)); }

#define BAR() asm volatile("s_waitcnt lgkmcnt(0)\n\ts_barrier" ::: "memory")
#define VM0() asm volatile("s_waitcnt vmcnt(0)" ::: "memory")
#define FENCE() asm volatile("" ::: "memory")
#define SCHED() __builtin_amdgcn_sched_barrier(0)
#define MFMA(acc, a, b) asm volatile("v_mfma_f32_16x16x32_bf16 %0, %1, %2, %0" : "+v"(acc) : "v"(a), "v"(b))
#define MFMA0(acc, a, b) asm volatile("v_mfma_f32_16x16x32_bf16 %0, %1, %2, 0" : "=&v"(acc) : "v"(a), "v"(b))

template <int DST, int IMM> DI void dmac(u32 voff, rsrc_t rs, u32 soff) {
  asm volatile("s_mov_b32 m0, %3\n\ts_nop 0\n\tbuffer_load_dwordx4 %0, %1, %2 offen offset:%4 lds" ::"v"(voff), "s"(rs), "s"(soff), "i"(DST - IMM), "i"(IMM) : "memory");
}
template <int DC, int IMM> DI void dmar(u32 voff, rsrc_t rs, u32 soff, u32 db) {
  asm volatile("s_add_u32 m0, %3, %4\n\ts_nop 0\n\tbuffer_load_dwordx4 %0, %1, %2 offen offset:%5 lds" ::"v"(voff), "s"(rs), "s"(soff), "s"(db), "i"(DC - IMM), "i"(IMM) : "memory", "scc");
}
template <int OFF> DI void touch(u32& sink, u32 vo, rsrc_t rs, u32 so) { asm volatile("buffer_load_dword %0, %1, %2, %3 offen offset:%4" : "+v"(sink) : "v"(vo), "s"(rs), "s"(so), "i"(OFF) : "memory"); }
template <int OFF> DI void touch1(u32& sink, u32 vo, rsrc_t rs, u32 so) { asm volatile("buffer_load_dword %0, %1, %2, %3 offen offset:%4 sc1" : "+v"(sink) : "v"(vo), "s"(rs), "s"(so), "i"(OFF) : "memory"); }
DI void pin2(u32& a, u32& b) { asm volatile("" : "+s"(a), "+s"(b)); }
template <int V> DI void wait_v() { asm volatile("s_waitcnt vmcnt(%0)" ::"i"(V) : "memory"); }
template <int OFF> DI void dsrd(v4u& d, u32 a) { asm volatile("ds_read_b128 %0, %1 offset:%2" : "=v"(d) : "v"(a), "i"(OFF)); }
template <int OFF> DI void bld(v4u& d, u32 vo, rsrc_t rs, u32 so) { asm volatile("buffer_load_dwordx4 %0, %1, %2, %3 offen offset:%4 sc1" : "=v"(d) : "v"(vo), "s"(rs), "s"(so), "i"(OFF)); }
template <int OFF> DI void bst(v4u d, u32 vo, rsrc_t rs, u32 so) { asm volatile("buffer_store_dwordx4 %0, %1, %2, %3 offen offset:%4 sc1" ::"v"(d), "v"(vo), "s"(rs), "s"(so), "i"(OFF) : "memory"); }
template <int OFF> DI void bstp(v4u d, u32 vo, rsrc_t rs, u32 so) { asm volatile("buffer_store_dwordx4 %0, %1, %2, %3 offen offset:%4 sc1\n\ts_nop 1" ::"v"(d), "v"(vo), "s"(rs), "s"(so), "i"(OFF) : "memory"); }
DI u32 cvt2(float a, float b) { u32 r; asm volatile("v_cvt_pk_bf16_f32 %0, %1, %2" : "=v"(r) : "v"(a), "v"(b)); return r; }
template <int V, int G> DI void wait_vl() { asm volatile("s_waitcnt vmcnt(%0) lgkmcnt(%1)" ::"i"(V), "i"(G)); }
template <bool Z> DI void mfma(f32x4& acc, const v4u& a, const v4u& b) { if constexpr (Z) MFMA0(acc, a, b); else MFMA(acc, a, b); }
template <int OFF> DI void dswr(u32 a, v4u d) { asm volatile("ds_write_b128 %0, %1 offset:%2" ::"v"(a), "v"(d), "i"(OFF) : "memory"); }
template <int OFF> DI void mark(u32 a, u32 v) { asm volatile("ds_write_b32 %0, %1 offset:%2" ::"v"(a), "v"(v), "i"(OFF) : "memory"); }
template <int OFF, int V> DI void marki(u32 a) { u32 t; asm volatile("v_mov_b32 %0, %3\n\tds_write_b32 %1, %0 offset:%2" : "=&v"(t) : "v"(a), "i"(OFF), "i"(V) : "memory"); }
template <int G> DI void wait_l() { asm volatile("s_waitcnt lgkmcnt(%0)" ::"i"(G)); }


template <typename T> DI T ldsld(LAS unsigned char* L, u32 off) { return *(LAS T*)(L + off); }
template <typename T> DI void ldsst(LAS unsigned char* L, u32 off, T v) { *(LAS T*)(L + off) = v; }
DI u32 lds_inc(LAS unsigned char* L, u32 off) { return __hip_atomic_fetch_add((LAS u32*)(L + off), 1u, __ATOMIC_RELAXED, __HIP_MEMORY_SCOPE_WORKGROUP); }
DI u32 lds_peek(LAS unsigned char* L, u32 off) { return __hip_atomic_load((LAS u32*)(L + off), __ATOMIC_RELAXED, __HIP_MEMORY_SCOPE_WORKGROUP); }
DI u32 lds_wait_ge(LAS unsigned char* L, u32 off, u32 v) {
  u32 r = 0;
  _Pragma("unroll 1") for (int sp = 0; sp < SPIN_L; ++sp) {
    FENCE();
    r = __builtin_amdgcn_readfirstlane(lds_peek(L, off));
    if (r >= v) break;
  }
  FENCE();
  return r;
}

DI void wait_words(LAS unsigned char* L, u32 off, u32 v) {
  _Pragma("unroll 1") for (int sp = 0; sp < SPIN_L; ++sp) {
    FENCE();
    if (__all(lds_peek(L, off) >= v)) break;
    __builtin_amdgcn_s_sleep(1);
  }
  FENCE();
}

DI void ln1(u32 l0, u32 l1, f32x2 (&n)[14][4]) {
  v4u xr[14];
  static_for<0, 14>([&](auto K) { dsrd<(K.value >> 1) * 1024>(xr[K.value], (K.value & 1) ? l1 : l0); });
  float s = 0.f, q = 0.f;
  const u32 one2 = 0x3f803f80u;
  static_for<0, 14>([&](auto K) {
    constexpr int k = K.value;
    if constexpr (!(k & 1)) { v4u t0 = xr[k], t1 = xr[k + 1]; asm volatile("s_waitcnt lgkmcnt(%2)" : "+v"(t0), "+v"(t1) : "i"(12 - k)); xr[k] = t0; xr[k + 1] = t1; }
    float a = s, c = q;
    const v4u t = xr[k];
    asm volatile("v_dot2_f32_bf16 %0, %2, %6, %0\n\tv_dot2_f32_bf16 %1, %2, %2, %1\n\tv_dot2_f32_bf16 %0, %3, %6, %0\n\tv_dot2_f32_bf16 %1, %3, %3, %1\n\t"
                 "v_dot2_f32_bf16 %0, %4, %6, %0\n\tv_dot2_f32_bf16 %1, %4, %4, %1\n\tv_dot2_f32_bf16 %0, %5, %6, %0\n\tv_dot2_f32_bf16 %1, %5, %5, %1"
                 : "+v"(a), "+v"(c) : "v"(t[0]), "v"(t[1]), "v"(t[2]), "v"(t[3]), "s"(one2));
    s = a; q = c;
  });
  asm volatile("s_nop 2" : "+v"(s), "+v"(q));
  float v = h32(s, q);
  v = h16(v, v);
  const auto r = __builtin_amdgcn_permlane32_swap(__float_as_uint(v), __float_as_uint(v), false, false);
  const float mu = __uint_as_float(r[0]) * (1.f / 448.f);
  const float rs = __builtin_amdgcn_rsqf(fmaxf(__uint_as_float(r[1]) * (1.f / 448.f) - mu * mu, 0.f) + 1e-5f);
  f32x2 rn = {rs, -mu * rs};
  asm volatile("s_nop 0" : "+v"(rn));
  static_for<0, 14>([&](auto K) {
    constexpr int k = K.value;
#pragma unroll
    for (int e = 0; e < 4; ++e) {
      const f32x2 t = {lo16(xr[k][e]), hi16(xr[k][e])}, r2 = rn;
      f32x2 o;
      asm volatile("v_pk_fma_f32 %0, %1, %2, %2 op_sel:[0,0,1] op_sel_hi:[1,0,1]" : "=v"(o) : "v"(t), "v"(r2));
      n[k][e] = o;
    }
    SCHED();
  });
}
DI void spin_words(u32 a) {
  _Pragma("unroll 1") for (int sp = 0; sp < SPIN_L; ++sp) {
    u32 t;
    asm volatile("ds_read_b32 %0, %1\n\ts_waitcnt lgkmcnt(0)" : "=v"(t) : "v"(a) : "memory");
    if (__all(t != 0)) break;
  }
}

DI v4u h_step(const f32x2 (&n)[4], const v4u (&q)[4]) {
  f32x2 t0 = n[0], t1 = n[1], t2 = n[2], t3 = n[3];
  u32 h0, h1, h2, h3;
  asm volatile("v_pk_fma_f32 %0, %0, %4, %8\n\tv_pk_fma_f32 %1, %1, %5, %9\n\tv_pk_fma_f32 %2, %2, %6, %10\n\tv_pk_fma_f32 %3, %3, %7, %11"
               : "+v"(t0), "+v"(t1), "+v"(t2), "+v"(t3)
               : "v"(v2u{q[0][0], q[0][1]}), "v"(v2u{q[0][2], q[0][3]}), "v"(v2u{q[1][0], q[1][1]}), "v"(v2u{q[1][2], q[1][3]}),
                 "v"(v2u{q[2][0], q[2][1]}), "v"(v2u{q[2][2], q[2][3]}), "v"(v2u{q[3][0], q[3][1]}), "v"(v2u{q[3][2], q[3][3]}));
  asm volatile("v_cvt_pk_bf16_f32 %0, %4, %5\n\tv_cvt_pk_bf16_f32 %1, %6, %7\n\tv_cvt_pk_bf16_f32 %2, %8, %9\n\tv_cvt_pk_bf16_f32 %3, %10, %11"
               : "=&v"(h0), "=&v"(h1), "=&v"(h2), "=&v"(h3) : "v"(t0[0]), "v"(t0[1]), "v"(t1[0]), "v"(t1[1]), "v"(t2[0]), "v"(t2[1]), "v"(t3[0]), "v"(t3[1]));
  return v4u{h0, h1, h2, h3};
}
DI void small_gemm(rsrc_t rw, const f32x2 (&n)[14][4], u32 w0, u32 w1, u32 w2, u32 w3, u32 cw, u32 fw, u32 vo0, u32 vo1, u32 so0, u32 so1) {
  v4u W[WD + 1], cs[3][4], h[14];
  u32 wl = 0;
  auto rdw = [&](auto M) { constexpr int m = M.value, k = m / 7, e = m % 7; dsrd<(e & 3) * 14336 + (k >> 1) * 1024>(W[m % (WD + 1)], e < 4 ? ((k & 1) ? w1 : w0) : ((k & 1) ? w3 : w2)); };
  auto rdc = [&](auto K) { static_for<0, 4>([&](auto I) { constexpr int k = K.value, i = I.value; dsrd<k * 128 + (i & 1) * 16 + (i >> 1) * 1792>(cs[k % 3][i], cw); }); };
  _Pragma("unroll 1") for (int sp = 0; sp < SPIN_L; ++sp) {
    u32 t;
    asm volatile("ds_read_b32 %0, %5 offset:%7\n\tds_read_b128 %1, %6\n\tds_read_b128 %2, %6 offset:16\n\tds_read_b128 %3, %6 offset:1792\n\tds_read_b128 %4, %6 offset:1808\n\ts_waitcnt lgkmcnt(4)"
                 : "=&v"(t), "=&v"(cs[0][0]), "=&v"(cs[0][1]), "=&v"(cs[0][2]), "=&v"(cs[0][3]) : "v"(L_CTL), "v"(cw), "i"(C_CS) : "memory");
    if (__builtin_amdgcn_readfirstlane(t)) break;
    wait_l<0>();
  }
  rdc(IC<1>{}); rdc(IC<2>{});
  static_for<0, 14>([&](auto K) {
    constexpr int k = K.value;
    if constexpr (k < 12) wait_l<(k == 9 || k == 10 ? 9 : 8)>();
    if constexpr (k == 12) wait_l<4 + WD / 2>();
    if constexpr (k == 13) wait_l<WD>();
    h[k] = h_step(n[k], cs[k % 3]);
    if constexpr (k + 3 < 14) rdc(IC<k + 3>{});
    if constexpr (k == 8) asm volatile("ds_read_b32 %0, %1" : "=v"(wl) : "v"(fw));
    if constexpr (k == 11) { asm volatile("" : "+v"(wl)); if (!__all(wl != 0)) spin_words(fw); static_for<0, WD / 2>(rdw); }
    if constexpr (k == 12) static_for<WD / 2, WD>(rdw);
  });
  f32x4 acc[7];
  static_for<0, 98>([&](auto M) {
    constexpr int m = M.value, k = m / 7, e = m % 7;
    if constexpr (m % 3 == 0) { constexpr int last = m + 2 < 97 ? m + 2 : 97, top = m + WD - 1 < 97 ? m + WD - 1 : 97; wait_l<top - last>(); }
    mfma<k == 0>(acc[e], h[k], W[m % (WD + 1)]);
    if constexpr (m + WD < 98) rdw(IC<m + WD>{});
  });
  static_for<0, 4>([&](auto I) {
    constexpr int i = I.value, e = 2 * i;
    if constexpr (i == 2) asm volatile("s_nop 1");
    const u32 p0 = cvt2(acc[e][0], acc[e][1]), p1 = cvt2(acc[e][2], acc[e][3]);
    u32 q0 = p0, q1 = p1;
    if constexpr (e + 1 < 7) { q0 = cvt2(acc[e + 1][0], acc[e + 1][1]); q1 = cvt2(acc[e + 1][2], acc[e + 1][3]); }
    const auto r0 = __builtin_amdgcn_permlane32_swap(p0, q0, false, false), r1 = __builtin_amdgcn_permlane32_swap(p1, q1, false, false);
    bstp<(i & 1) * 2048>(v4u{r0[0], r1[0], r0[1], r1[1]}, i == 3 ? vo1 : vo0, rw, i < 2 ? so0 : so1);
  });
}

template <int W> struct Part {
  static constexpr int NB = W == 3 ? 1 : 2, E0 = 2 * W, NT = 8 * NB;
  static constexpr DI int tn(int m) { return m >> 3; }
  static constexpr DI int tt(int m) { return m & 7; }
};
static constexpr DI int slice_of(int pos) { return 7 - (pos >> 1) + 8 * (pos & 1); }
static constexpr DI int kblk(int ks) { return 2 * slice_of(ks >> 1) + (ks & 1); }
DI void ctl_wait(u32 ctl, u32& have, u32& hx, u32 na, u32 nx) {
  _Pragma("unroll 1") for (int sp = 0; sp < SPIN_L; ++sp) {
    v2u t;
    asm volatile("ds_read_b64 %0, %1 offset:%2\n\ts_waitcnt lgkmcnt(0)" : "=v"(t) : "v"(ctl), "i"(C_FULL) : "memory");
    have = __builtin_amdgcn_readfirstlane(t.x); hx = __builtin_amdgcn_readfirstlane(t.y);
    if (have >= na && hx >= nx) break;
  }
}

template <int W>
DI void big_gemm(const Params& p, LAS unsigned char* L, int lane, u32 bsoff, u32 psoff) {
  using P = Part<W>;
  constexpr int NB = P::NB, NT = P::NT;
  const rsrc_t rw = mk(p.ws, WS_SIZE);
  const int r = lane & 15, g = lane >> 4;
  const u32 l0 = (r >> 3) * 1024 + (r & 7) * 128 + ((g ^ (r & 7)) * 16), vo = lane * 16;
  u32 lb[3][2];
#pragma unroll
  for (int i = 0; i < 3; ++i) { lb[i][0] = l0 + i * 65536; lb[i][1] = (l0 ^ 64) + i * 65536; asm volatile("" : "+v"(lb[i][0]), "+v"(lb[i][1])); }
  u32 ctl = L_CTL, pv = 0, bs = bsoff + P::E0 * 1024 + kblk(0) * 28672;
  v2u fv = {0, 0};
  asm volatile("" : "+v"(ctl));
  u32 have = 0, hx = 0;
  asm volatile("" : "+s"(bs));
  ctl_wait(ctl, have, hx, 0, XST);
  v4u B[PD + 1][NB], A[2][8];
  static_for<0, PD>([&](auto S) {
    if constexpr (S.value / 2 >= XST) { if (hx <= S.value / 2) ctl_wait(ctl, have, hx, 0, S.value / 2 + 1); }
    static_for<0, NB>([&](auto N) { bld<N.value * 1024>(B[S.value][N.value], vo, rw, bs); });
    asm volatile("s_add_u32 %0, %0, %1" : "+s"(bs) : "i"((kblk(S.value + 1) - kblk(S.value)) * 28672) : "scc");
  });
  if (have < 1) ctl_wait(ctl, have, hx, 1, 0);
  static_for<0, 8>([&](auto T) { dsrd<T.value * 2048>(A[0][T.value], lb[0][0]); });
  f32x4 acc[NT];
  static_for<0, 32>([&](auto K) {
    constexpr int ks = K.value, k1 = ks + 1, sl = (k1 >> 1) % 9, left = 31 - ks;
    wait_vl<NB*(left < PD - 1 ? left : PD - 1), 4>();
    static_for<0, (NT > 14 ? NT : 14)>([&](auto M) {
      constexpr int m = M.value, ks = K.value, kp = ks + PD;
      if constexpr (m == 4) wait_l<(k1 < 32 ? 4 : 0)>();
      if constexpr (m < NT) mfma<ks == 0>(acc[m], B[ks % (PD + 1)][P::tn(m)], A[ks & 1][P::tt(m)]);
      if constexpr (m < 8 && k1 < 32) dsrd<(sl & 3) * 16384 + m * 2048>(A[k1 & 1][m], lb[sl >> 2][k1 & 1]);
      if constexpr (m == 5 && !(ks & 1) && ks > 0 && ks < 30) { asm volatile("" : "+v"(fv)); have = __builtin_amdgcn_readfirstlane(fv.x); if (ks + PD < 33) hx = __builtin_amdgcn_readfirstlane(fv.y); }
      if constexpr (m == 7 && kp < 32 && (!(kp & 1) || ks == 0)) { if (hx <= kp / 2) ctl_wait(ctl, have, hx, 0, kp / 2 + 1); }
      if constexpr (m >= 8 && m < 8 + NB && kp < 32) bld<(m - 8) * 1024>(B[kp % (PD + 1)][m - 8], vo, rw, bs);
      if constexpr (m == 11 && kp + 1 < 32) asm volatile("s_add_u32 %0, %0, %1" : "+s"(bs) : "i"((kblk(kp + 1) - kblk(kp)) * 28672) : "scc");
      constexpr int pr = (ks & 1) && ks <= 13 ? G_RING + ks / 2 : ks == FFNKS ? G_FFN : 0;
      if constexpr (m == 11 && pr) asm volatile("v_mov_b32 %0, %1" : "=v"(pv) : "i"(pr));
      if constexpr (m == 12 && pr) mark<C_PROG + W * 4>(ctl, pv);
      if constexpr (m == 13 && (ks & 1) && ks < 29) asm volatile("ds_read_b64 %0, %1 offset:%2" : "=v"(fv) : "v"(ctl), "i"(C_FULL));
      if constexpr (m == 13 && !(ks & 1) && ks < 30) { if (have < ks / 2 + 2) ctl_wait(ctl, have, hx, ks / 2 + 2, 0); }
    });
  });
  asm volatile("s_nop 7\n\ts_nop 3");
  const u32 qo = (r >> 2) * 28672 + g * 64 + (r & 3) * 16;
  static_for<0, NT / 2>([&](auto I) {
    constexpr int m = 2 * I.value;
    const u32 o0 = cvt2(acc[m][0], acc[m][1]), o1 = cvt2(acc[m][2], acc[m][3]), o2 = cvt2(acc[m + 1][0], acc[m + 1][1]), o3 = cvt2(acc[m + 1][2], acc[m + 1][3]);
    bstp<(P::E0 + P::tn(m)) * 256>(v4u{o0, o1, o2, o3}, qo, rw, psoff + (P::tt(m) >> 1) * 4 * 28672);
  });
}

DI void flag_after_drain(rsrc_t rw, u32 vf, u32 ep) {
  asm volatile("s_waitcnt vmcnt(0)\n\tbuffer_store_dword %0, %1, %2, 0 offen sc1" ::"v"(ep), "v"(vf), "s"(rw) : "memory");
}
template <int CNT> DI void flag_last(rsrc_t rw, u32 vf, u32 ep) {
  u32 old, a = L_CTL, one = 1;
  u64 ex;
  asm volatile("s_mov_b64 %1, exec\n\ts_mov_b64 exec, 1\n\ts_waitcnt vmcnt(0)\n\tds_add_rtn_u32 %0, %2, %3 offset:%4\n\ts_mov_b64 exec, %1\n\ts_waitcnt lgkmcnt(0)" : "=&v"(old), "=&s"(ex) : "v"(a), "v"(one), "i"(CNT) : "memory");
  if (__builtin_amdgcn_readfirstlane(old) == 3) asm volatile("buffer_store_dword %0, %1, %2, 0 offen sc1" ::"v"(ep), "v"(vf), "s"(rw) : "memory");
}
DI void poll64(const Params& p, u32 off, u32 ep, int lane, u32 po) {
  const rsrc_t rw = mk(p.ws, WS_SIZE);
  u32 vo = (lane >> 2) * 128, sink = 0;
  const u32 q = lane / 14, vt = lane < 56 ? lane * 128 : 0x80000000u;
  u64 seen = 0, done = 0;
  _Pragma("unroll 1") for (int sp = 0; sp < SPIN_G; ++sp) {
    u32 v;
    asm volatile("buffer_load_dword %0, %2, %3, %4 offen sc1\n\ts_waitcnt vmcnt(0)" : "=&v"(v), "+v"(sink) : "v"(vo), "s"(rw), "s"(off) : "memory");
    seen |= __ballot(v == ep);
    if (seen == ~0ull) break;
    if (XPT) {
      vo = v == ep ? 0x80000000u : vo;
      u64 f = seen & (seen >> 1); f &= f >> 2; f &= 0x1111111111111111ull;
      const u64 fresh = f & ~done;
      done = f;
      static_for<0, 4>([&](auto I) {
        const u32 m = (u32)(fresh >> (16 * I.value)) & 0xffff;
        if (m) touch1<0>(sink, (m >> (4 * q)) & 1 ? vt : 0x80000000u, rw, po + I.value * 7168);
      });
    }
    __builtin_amdgcn_s_sleep(PSLEEP);
  }
  asm volatile("s_waitcnt vmcnt(0)" : "+v"(sink)::"memory");
  FENCE();
}
DI void poll_ab(const Params& p, u32 off, u32 ep, int lane) {
  const rsrc_t rw = mk(p.ws, WS_SIZE);
  u32 vo = slice_of(lane >> 2) * 128, cnt = 0;
  u64 seen = 0;
  _Pragma("unroll 1") for (int sp = 0; sp < SPIN_G; ++sp) {
    FENCE();
    const bool ok = __builtin_amdgcn_raw_buffer_load_b32(rw, PWIN < 16 && (lane >> 2) >= cnt + PWIN ? 0x80000000u : vo, off, SC1) == ep && (PWIN >= 16 || ((lane >> 2) < cnt + PWIN && (int)vo >= 0));
    seen |= __ballot(ok);
    vo = ok ? 0x80000000u : vo;
    const u32 n = seen == ~0ull ? 16 : __builtin_ctzll(~seen) >> 2;
    if (n > cnt) { cnt = n; mark<C_X1>(L_CTL, cnt); }
    if (cnt == 16) break;
    __builtin_amdgcn_s_sleep(PSLEEP);
  }
  if (cnt < 16) mark<C_X1>(L_CTL, 16);
  FENCE();
}
DI void wait_prog(LAS unsigned char* L, u32 v, int lane) { wait_words(L, L_CTL + C_PROG + (lane & 3) * 4, v); }
DI void poll_cs(const Params& p, LAS unsigned char* L, int b, u32 ep, int lane) {
  const rsrc_t rw = mk(p.ws, WS_SIZE);
  v4u g[4];
  u32 v0 = lane * 16, v1 = min(lane, 47) * 16 + 1024, so = WS_MOD + (b & 7) * MODREP, a = L_MOD + lane * 16;
  asm volatile("" : "+v"(v0), "+v"(v1), "+v"(a));
  asm volatile("" : "+s"(so));
  _Pragma("unroll 1") for (int sp = 0; sp < SPIN_G; ++sp) {
    FENCE();
    bool ok = true;
#pragma unroll
    for (int k = 0; k < 4; ++k) g[k] = __builtin_amdgcn_raw_buffer_load_b128(rw, ((k & 1) ? v1 : v0) + (k >> 1) * 1792, so, SC1);
#pragma unroll
    for (int k = 0; k < 4; ++k) ok &= g[k].y == ep && g[k].w == ep;
    if (__all(ok)) break;
  }
  auto U = [](v4u t) { return v4u{t.x << 16, t.x & 0xffff0000u, t.z << 16, t.z & 0xffff0000u}; };
  dswr<0>(a, U(g[2])); dswr<1792>(a, U(g[0]));
  if (lane < 48) { dswr<1024>(a, U(g[3])); dswr<1792 + 1024>(a, U(g[1])); }
  marki<C_CS, 1>(L_CTL);
}
DI void poll_rest(const Params& p, LAS unsigned char* L, int b, u32 ep, int lane) {
  const rsrc_t rw = mk(p.ws, WS_SIZE);
  v4u g[7];
  _Pragma("unroll 1") for (int sp = 0; sp < SPIN_G; ++sp) {
    FENCE();
    bool ok = true;
#pragma unroll
    for (int k = 0; k < 7; ++k) g[k] = __builtin_amdgcn_raw_buffer_load_b128(rw, lane * 16 + (k & 3) * 1024, WS_MOD + (b & 7) * MODREP + 3584 + (k >> 2) * 4096, SC1);
#pragma unroll
    for (int k = 0; k < 7; ++k) ok &= g[k].y == ep && g[k].w == ep;
    if (__all(ok)) break;
  }
  wait_prog(L, G_H, lane);
#pragma unroll
  for (int k = 0; k < 7; ++k) ldsst<v2u>(L, L_MOD + 1792 + (k * 64 + lane) * 8, v2u{g[k].x, g[k].z});
  FENCE();
}
DI u32 poll_keys(const Params& p, u32 ep, int lane, u32 ko) {
  const rsrc_t rw = mk(p.ws, WS_SIZE);
  u32 best = 0, vo[4];
#pragma unroll
  for (int i = 0; i < 4; ++i) vo[i] = ((i * 64 + lane) / KPL) * 128 + (lane % KPL) * 8;
  _Pragma("unroll 1") for (int sp = 0; sp < SPIN_G; ++sp) {
    FENCE();
    v2u k[4];
#pragma unroll
    for (int i = 0; i < 4; ++i) k[i] = __builtin_amdgcn_raw_buffer_load_b64(rw, vo[i], ko, SC1);
#pragma unroll
    for (int i = 0; i < 4; ++i) { const bool v = k[i].y == ep && (int)vo[i] >= 0; best = v ? umax(best, k[i].x) : best; vo[i] = v ? 0x80000000u : vo[i]; }
    if (__all((int)(vo[0] & vo[1] & vo[2] & vo[3]) < 0)) break;
    __builtin_amdgcn_s_sleep(KSL);
  }
  FENCE();
  best = umax(best, dppu<XOR1>(best)); best = umax(best, dppu<XOR2>(best)); best = umax(best, dppu<HMIR>(best)); best = umax(best, dppu<MIR>(best));
  best = max16(best); best = max32(best);
  return __builtin_amdgcn_readfirstlane(best);
}

struct TailIn { v2u xa, xb, wr; };
DI void tail_issue(const Params& p, int b, int w, int lane, TailIn& t) {
  const rsrc_t rx = mk(p.x, 2048 * 896), rr = mk(p.wr, 896);
  const u32 e2 = 128 * w + 8 * (lane >> 2), vx = (lane & 3) * 896 + e2, so = b * 7168;
  asm volatile("buffer_load_dwordx2 %0, %1, %2, %3 offen" : "=v"(t.xa) : "v"(vx), "s"(rx), "s"(so) : "memory");
  asm volatile("buffer_load_dwordx2 %0, %1, %2, %3 offen offset:3584" : "=v"(t.xb) : "v"(vx), "s"(rx), "s"(so) : "memory");
  asm volatile("buffer_load_dwordx2 %0, %1, %2, 0 offen" : "=v"(t.wr) : "v"(e2), "s"(rr) : "memory");
}

struct CAddr { u32 w0, w1, w2, w3, l0, l1, cs, vo0, vo1, vf1, vf2; };
typedef u32 v16u __attribute__((ext_vector_type(16)));
typedef u32 v8u __attribute__((ext_vector_type(8)));
template <typename T> DI T* ptr(u32 lo, u32 hi) { return (T*)(((u64)hi << 32) | lo); }

__global__ void __launch_bounds__(512, 2) k_fused(Params) {
  extern __shared__ __attribute__((aligned(16))) unsigned char smem[];
  LAS unsigned char* L = (LAS unsigned char*)smem;
  v16u ka; v8u kb; v2u kc;
  asm volatile("s_load_dwordx8 %1, %3, 0x40\n\ts_load_dwordx16 %0, %3, 0x0\n\ts_load_dwordx2 %2, %3, 0x60" : "=&s"(ka), "=&s"(kb), "=&s"(kc) : "s"(__builtin_amdgcn_kernarg_segment_ptr()));
  SCHED();
  const int b = blockIdx.x, lane = threadIdx.x & 63, w = __builtin_amdgcn_readfirstlane(threadIdx.x >> 6);
  const int xcd = b & 7, j = b >> 3;
  const int half = xcd & 1, ec = j & 3;
  const int sig = (xcd >> 1) * 8 + (j >> 2);
  const int kh = (xcd >> 1) & 1, tt = (xcd >> 2) * 8 + (j >> 2);
  const int xc = xcd;
  const int cg = (j >> 2) * 32 + xc * 4 + ec;
  const bool prod = cg < 168;
  const int lw = prod || xc < 2 ? 56 : 38;
  u32 vt = prod ? lane * 5376 : min((((j >> 2) - (xc < 2 ? 6 : 5)) * 7 + w) * lw + lane, 783) * 128;
  if (lane >= lw || (prod ? (w & 6) != 4 : w == 7) || !((1 >> (prod ? 0 : 1)) & 1)) vt = 0x80000000u;
  u32 stc = prod ? (ec * 112 + 56 * (w & 1)) * 5376 + (cg >> 2) * 128 : ec * 100352;
  u32 ntc = prod ? 448 * 5376 : 448 * 896;
  u64 pc0; u32 pcn;
  u32 pcm;
  asm volatile("s_getpc_b64 %0\n.Lpc_first:\n\ts_mov_b32 %1, .Lpc_last-.Lpc_first\n\ts_mov_b32 %2, .Lpc_mid-.Lpc_first" : "=s"(pc0), "=s"(pcn), "=s"(pcm));
  u32 vpc = lane < 20 ? (lane * 32 + j) * 128 : 0x80000000u;
  u32 vm = (128 * (w & 3) + 2 * (lane & 62)) * 5376 + (lane & 1) * 16, vc = 256 * (w & 3) + (lane & 62) * 4, sm = cg * 32, sm1 = sm + 5376, sm2 = sm + 2 * 5376, sm3 = sm + 3 * 5376;
  asm volatile("" : "+s"(sm2), "+s"(sm3));
  const u32 r8 = lane >> 3;
  u32 vx = (w < 4 ? r8 + (r8 & 4) : r8) * 896 + (((lane & 7) ^ r8) * 16), pw = L_CTL + w * 4;
  u32 sx = (sig * 64 + 16 * w + 4 * (ec & 1)) * 896, dx = L_H + w * 14336 + (ec & 1) * 7168;
  pin2(sx, dx);
  asm volatile("" : "+v"(pw));
  if (w == 7) ldsst<u32>(L, L_CTL + lane * 4, 0);
  asm volatile("" : "+v"(vm), "+v"(vc), "+v"(vx), "+v"(vt));
  asm volatile("s_waitcnt lgkmcnt(0)" : "+s"(ka), "+s"(kb), "+s"(kc), "+s"(sm), "+s"(sm1), "+s"(stc), "+s"(ntc)::"memory");
  Params p;
  p.x = ptr<const u16>(ka[0], ka[1]); p.sre = ptr<const u16>(ka[2], ka[3]); p.sim = ptr<const u16>(ka[4], ka[5]); p.dre = ptr<const u16>(ka[6], ka[7]);
  p.dim = ptr<const u16>(ka[8], ka[9]); p.wr = ptr<const u16>(ka[10], ka[11]); p.wg = ptr<const u16>(ka[12], ka[13]); p.wu = ptr<const u16>(ka[14], ka[15]);
  p.wd = ptr<const u16>(kb[0], kb[1]); p.c = ptr<const u16>(kb[2], kb[3]); p.wmod = ptr<const u16>(kb[4], kb[5]); p.out = ptr<u16>(kb[6], kb[7]);
  p.ws = ptr<unsigned char>(kc[0], kc[1]);
  const rsrc_t rw = mk(p.ws, WS_SIZE);

  const u16* const wdh = half ? p.dim : p.dre;
  u32 junk = 0;
  touch<0>(junk, vt, mk(prod ? p.wmod : wdh, ntc), stc);
  v4u wm[4]; v2u cv;
  const bool mw = prod && w >= 4;
  if (mw) {
    const rsrc_t rm = mk(p.wmod, 448 * 2688 * 2), rc = mk(p.c, 896);
    const u32 s0 = sm, s1 = sm1, s2 = sm2, s3 = sm3;
    asm volatile("buffer_load_dwordx2 %0, %1, %2, 0 offen" : "=v"(cv) : "v"(vc), "s"(rc) : "memory");
    asm volatile("buffer_load_dwordx4 %0, %1, %2, %3 offen" : "=v"(wm[0]) : "v"(vm), "s"(rm), "s"(s0) : "memory");
    asm volatile("buffer_load_dwordx4 %0, %1, %2, %3 offen" : "=v"(wm[1]) : "v"(vm), "s"(rm), "s"(s1) : "memory");
    asm volatile("buffer_load_dwordx4 %0, %1, %2, %3 offen" : "=v"(wm[2]) : "v"(vm), "s"(rm), "s"(s2) : "memory");
    asm volatile("buffer_load_dwordx4 %0, %1, %2, %3 offen" : "=v"(wm[3]) : "v"(vm), "s"(rm), "s"(s3) : "memory");
  }
  if (w == CPW) touch<0>(junk, vpc, mk((const void*)pc0, pcn), 0);
  asm volatile("s_barrier" ::: "memory");
  u32 ep0;
  asm volatile("s_load_dword %0, %1, 0x0" : "=s"(ep0) : "s"(p.ws));
  auto group = [&](rsrc_t rs, u32 so, u32 db) { static_for<0, 7>([&](auto C) { dmar<C.value * 1024, C.value * 128>(vx, rs, so, db); }); };
  if (w < 4) {
    const rsrc_t rx = mk(p.x, 2048 * 896);
    group(rx, sx, dx);
    group(rx, sx + ((ec & 1) ? -3584 : 3584), dx + ((ec & 1) ? -7168 : 7168));
  }
  if (mw) {
    u32 ar = L_RED + (w & 3) * 64 + (lane & 1) * 32 + (lane >> 5) * 16 + ((lane >> 4) & 1) * 8;
    asm volatile("s_waitcnt vmcnt(4)" : "+v"(cv), "+v"(ar)::"memory");
    const u32 h0 = pk(0.f, siluf(lo16(cv.x))), h1 = pk(0.f, siluf(hi16(cv.x))), h2 = pk(0.f, siluf(lo16(cv.y))), h3 = pk(0.f, siluf(hi16(cv.y)));
    u32 hh[4] = {h0, h1, h2, h3}, ll[4] = {h0 >> 16, h1 >> 16, h2 >> 16, h3 >> 16};
    asm volatile("s_waitcnt vmcnt(0)" : "+v"(wm[0]), "+v"(wm[1]), "+v"(wm[2]), "+v"(wm[3]), "+v"(hh[0]), "+v"(hh[1]), "+v"(hh[2]), "+v"(hh[3]), "+v"(ll[0]), "+v"(ll[1]), "+v"(ll[2]), "+v"(ll[3])::"memory");
    float a[8];
#pragma unroll
    for (int q = 0; q < 4; ++q) {
      a[2 * q] = dot2(wm[3][q], ll[3], dot2(wm[2][q], ll[2], dot2(wm[1][q], ll[1], dot2(wm[0][q], ll[0], 0.f))));
      a[2 * q + 1] = dot2(wm[3][q], hh[3], dot2(wm[2][q], hh[2], dot2(wm[1][q], hh[1], dot2(wm[0][q], hh[0], 0.f))));
    }
    float v0 = h16(h32(a[0], a[4]), h32(a[2], a[6])), v1 = h16(h32(a[1], a[5]), h32(a[3], a[7]));
    v0 += dppf<ROR4>(v0); v1 += dppf<ROR4>(v1); v0 += dppf<ROR8>(v0); v1 += dppf<ROR8>(v1); v0 += dppf<XOR2>(v0); v1 += dppf<XOR2>(v1);
    if ((lane & 14) == 0) asm volatile("ds_write_b64 %0, %1" ::"v"(ar), "v"(f32x2{v0, v1}) : "memory");
    marki<C_M - 16, 1>(pw);
  }
  if (prod && w == 7) {
    u32 ar = L_RED + (lane & 7) * 8, vs = (lane >> 3) * MODREP + (lane & 7) * 8, ss = WS_MOD + cg * 64;
    asm volatile("" : "+v"(ar), "+v"(vs));
    asm volatile("" : "+s"(ss));
    spin_words(L_CTL + C_M + (lane & 3) * 4);
    u32 e1;
    asm volatile("s_add_u32 %0, %1, %2" : "=s"(e1) : "s"(ep0), "i"(EP_STEP) : "scc");
    f32x2 t0, t1, t2, t3;
    asm volatile("ds_read_b64 %0, %4\n\tds_read_b64 %1, %4 offset:64\n\tds_read_b64 %2, %4 offset:128\n\tds_read_b64 %3, %4 offset:192\n\ts_waitcnt lgkmcnt(0)" : "=&v"(t0), "=&v"(t1), "=&v"(t2), "=&v"(t3) : "v"(ar) : "memory");
    const f32x2 t = (t0 + t1) + (t2 + t3);
    u32 v = pk(t[0], t[1]);
    const int part = cg / 28;
    if (part == 1 || part == 4) v = pk(1.f + lo16(v), 1.f + hi16(v));
    __builtin_amdgcn_raw_buffer_store_b64(v2u{v, e1}, rw, vs, ss, SC1);
    marki<C_S - 28, 1>(pw);
  }
  SCHED();
  asm volatile("s_waitcnt lgkmcnt(0)" : "+s"(ep0)::"memory");
  const u32 ep = ep0 + EP_STEP;
  TailIn ti;

  if (w == 7) {
    u32 tsink = junk;
    const rsrc_t ra = mk(half ? p.sim : p.sre, 2048 * 4096);
    u32 va = (lane & 31) * 4096 + (lane >> 5) * 1024, sa = (tt * 128 + ec * 32) * 4096 + kh * 2048;
    const unsigned char* tp = lane < 56 ? (const unsigned char*)p.x + (b * 7168 + lane * 128) : (const unsigned char*)p.wr + (lane & 7) * 112;
    asm volatile("" : "+v"(tp), "+v"(va));
    asm volatile("" : "+s"(sa));
    spin_words(L_CTL + C_XL + min(lane & 3, 2) * 4);
    poll_cs(p, L, b, ep, lane);
    spin_words(L_CTL + C_WL + min(lane & 3, 2) * 4);
    static_for<0, TB>([&](auto I) { u32 k_ = tsink; touch<slice_of(2 * I.value) * 128>(k_, va, ra, sa); tsink = k_; });
    asm volatile("global_load_dword %0, %1, off" : "+v"(tsink) : "v"(tp) : "memory");
    poll_rest(p, L, b, ep, lane);
    asm volatile("s_waitcnt vmcnt(0)" : "+v"(tsink)::"memory");
    if (!prod) { wait_prog(L, G_X1, lane); __builtin_amdgcn_s_sleep(PD1); }
    poll_ab(p, WS_F1 + ((half * 4 + ec) * 32 + kh * 16) * 128, ep, lane);
    {
      const u32 vg = lane < 56 ? (lane >> 2) * 3584 + (lane & 3) * 128 : 0x80000000u, sg = j * 14 * 3584 + ((448 * xcd) & ~127);
      const u32 vd = lane < 49 ? lane * 128 : 0x80000000u, sd = xcd * 224 * 896 + j * 6272;
      wait_prog(L, G_FFN, lane);
      touch<0>(tsink, vg, mk(p.wg, 448 * 3584), sg); touch<0>(tsink, vg, mk(p.wu, 448 * 3584), sg); touch<0>(tsink, vd, mk(p.wd, 1792 * 896), sd);
      asm volatile("s_waitcnt vmcnt(0)" : "+v"(tsink)::"memory");
    }
    if (b < NFFN) {
      const int fg = (b & 7) * 14 + (b >> 3);
      wait_prog(L, G_X2, lane);
      u32 wgp[7][8], wup[7][8], wdp[8][8];
      {
        const rsrc_t rg = mk(p.wg, 448 * 3584), ru = mk(p.wu, 448 * 3584), rd = mk(p.wd, 1792 * 896);
        v4u tg[7][2], tu[7][2], td[8][2];
        const u32 vo = (lane >> 1) * 4 * 3584 + (lane & 1) * 16, vo6 = (lane >> 1) * 2 * 3584 + (lane & 1) * 16;
#pragma unroll
        for (int t = 0; t < 7; ++t)
#pragma unroll
          for (int k = 0; k < 2; ++k) {
            const u32 so = fg * 32 + (t < 6 ? 128 * (t >> 1) + 2 * (t & 1) + k : 384 + k) * 3584;
            tg[t][k] = __builtin_amdgcn_raw_buffer_load_b128(rg, t < 6 ? vo : vo6, so, AUX_FFN);
            tu[t][k] = __builtin_amdgcn_raw_buffer_load_b128(ru, t < 6 ? vo : vo6, so, AUX_FFN);
          }
        const u32 vd = lane < 56 ? lane * 16 : 0x80000000u;
#pragma unroll
        for (int t = 0; t < 8; ++t)
#pragma unroll
          for (int k = 0; k < 2; ++k) td[t][k] = __builtin_amdgcn_raw_buffer_load_b128(rd, vd, (fg * 16 + 2 * t + k) * 896, AUX_FFN);
        SCHED();
        asm volatile("s_barrier\n\ts_barrier" ::: "memory"); SCHED();
#pragma unroll
        for (int t = 0; t < 7; ++t)
#pragma unroll
          for (int q = 0; q < 4; ++q) {
            wgp[t][2 * q] = __builtin_amdgcn_perm(tg[t][1][q], tg[t][0][q], 0x05040100); wgp[t][2 * q + 1] = __builtin_amdgcn_perm(tg[t][1][q], tg[t][0][q], 0x07060302);
            wup[t][2 * q] = __builtin_amdgcn_perm(tu[t][1][q], tu[t][0][q], 0x05040100); wup[t][2 * q + 1] = __builtin_amdgcn_perm(tu[t][1][q], tu[t][0][q], 0x07060302);
            asm volatile("" : "+v"(wgp[t][2 * q]), "+v"(wgp[t][2 * q + 1]), "+v"(wup[t][2 * q]), "+v"(wup[t][2 * q + 1]));
          }
#pragma unroll
        for (int t = 0; t < 8; ++t)
#pragma unroll
          for (int q = 0; q < 4; ++q) {
            wdp[t][2 * q] = __builtin_amdgcn_perm(td[t][1][q], td[t][0][q], 0x05040100); wdp[t][2 * q + 1] = __builtin_amdgcn_perm(td[t][1][q], td[t][0][q], 0x07060302);
            asm volatile("" : "+v"(wdp[t][2 * q]), "+v"(wdp[t][2 * q + 1]));
          }
      }
      wait_words(L, L_CTL + C_H + min(lane & 7, PW - 1) * 4, 2);
      __builtin_amdgcn_s_sleep(PDLY);
      const u32 vf = lane < 56 ? (lane >> 2) * 2 * 7168 + (lane & 3) * 16 : 0x80000000u;
      const u32 key = poll_keys(p, ep, lane, WS_KEYR + xcd * 32768);
      const u32 cb = WS_CAND + ((2047 - (key & 0x7ff)) >> 3) * 3584;
      u32 hp[7];
      _Pragma("unroll 1") for (int sp = 0; sp < SPIN_G; ++sp) {
        FENCE();
        v4u q[3];
#pragma unroll
        for (int i = 0; i < 3; ++i) q[i] = __builtin_amdgcn_raw_buffer_load_b128(rw, (lane >> 1) * 16 + i * 512, cb, SC1);
        const v2u q6 = __builtin_amdgcn_raw_buffer_load_b64(rw, (lane >> 1) * 8 + 1536, cb, SC1);
        bool ok = q6.y == ep;
#pragma unroll
        for (int i = 0; i < 3; ++i) { ok &= q[i].y == ep && q[i].w == ep; hp[2 * i] = q[i].x; hp[2 * i + 1] = q[i].z; }
        hp[6] = q6.x;
        if (__all(ok)) break;
      }
      FENCE();
      {
        {
          float g[8], u[8];
#pragma unroll
          for (int n = 0; n < 8; ++n) { g[n] = 0.f; u[n] = 0.f; }
#pragma unroll
          for (int t = 0; t < 7; ++t)
#pragma unroll
            for (int n = 0; n < 8; ++n) { g[n] = dot2(wgp[t][n], hp[t], g[n]); u[n] = dot2(wup[t][n], hp[t], u[n]); }
          const float g0 = h16(h32(g[0], g[4]), h32(g[2], g[6])), g1 = h16(h32(g[1], g[5]), h32(g[3], g[7]));
          const float u0 = h16(h32(u[0], u[4]), h32(u[2], u[6])), u1 = h16(h32(u[1], u[5]), h32(u[3], u[7]));
          const bool b2 = lane & 4;
          const float gr1 = dppf<SHR4>(g1), gl0 = dppf<SHL4>(g0), ur1 = dppf<SHR4>(u1), ul0 = dppf<SHL4>(u0);
          float gg = b2 ? g1 + gr1 : g0 + gl0;
          float uu = b2 ? u1 + ur1 : u0 + ul0;
          gg += dppf<ROR8>(gg); uu += dppf<ROR8>(uu); gg += dppf<XOR2>(gg); uu += dppf<XOR2>(uu);
          const float act = rbf(siluf(rbf(gg))) * rbf(uu);
          const u32 ap = pk(act, dppf<SHL4>(act));
          float o[8];
#pragma unroll
          for (int n = 0; n < 8; ++n) o[n] = 0.f;
#pragma unroll
          for (int t = 0; t < 8; ++t) {
            const u32 s = __builtin_amdgcn_readlane(ap, (t >> 2) + ((t >> 1) & 1) * 32 + (t & 1) * 16);
#pragma unroll
            for (int n = 0; n < 8; ++n) o[n] = dot2(wdp[t][n], s, o[n]);
          }
          bstp<0>(v4u{pk(o[0], o[1]), ep, pk(o[2], o[3]), ep}, vf, rw, WS_FP + fg * 64);
          bstp<0>(v4u{pk(o[4], o[5]), ep, pk(o[6], o[7]), ep}, vf, rw, WS_FP + 7168 + fg * 64);
        }
      }
    } else {
      BAR();
      BAR();
      if (b < NFFN + NFIN) {
        const int f = b - NFFN, col = 32 * (f >> 1) + 8 * (lane & 3) + 4 * (f & 1);
        const v2u gb = ldsld<v2u>(L, L_MOD + 5 * 896 + col * 2);
      wait_words(L, L_CTL + C_H + min(lane & 7, PW - 1) * 4, 2);
      __builtin_amdgcn_s_sleep(PDLY);
        const u32 vz = col * 4 + 1792, vd = (f >> 2) * 4, vq = lane * 16;
        const u32 key = poll_keys(p, ep, lane, WS_KEYR + xcd * 32768);
        const u32 tn = (u32)__builtin_amdgcn_s_memrealtime();
        const u32 spec = (2047 - (key & 0x7ff)) >> 3, kb = (key & ~0x7ffu) | 0x400u;
        const float rv = __uint_as_float((kb & 0x80000000u) ? (kb & 0x7fffffffu) : ~kb);
        float sg = rbf(__builtin_amdgcn_rcpf(1.f + __expf(-rbf(rv))));
        asm volatile("" : "+v"(sg));
        _Pragma("unroll 1") for (int sp = 0; sp < 256 && (u32)__builtin_amdgcn_s_memrealtime() - tn < FD; ++sp) {}
        v4u z;
        float s0, s1, s2, s3;
        _Pragma("unroll 1") for (int sp = 0; sp < SPIN_G; ++sp) {
          FENCE();
          v4u q[7];
#pragma unroll
          for (int i = 0; i < 7; ++i) q[i] = __builtin_amdgcn_raw_buffer_load_b128(rw, vq, WS_FP + f * 7168 + i * 1024, SC1);
          z = __builtin_amdgcn_raw_buffer_load_b128(rw, vz, WS_CAND + spec * 3584, SC1);
          const u32 dn = __builtin_amdgcn_raw_buffer_load_b32(rw, vd, WS_DONE + spec * 128, SC1);
          bool ok = true; s0 = s1 = s2 = s3 = 0.f;
#pragma unroll
          for (int i = 0; i < 7; ++i) { ok &= q[i].y == ep && q[i].w == ep; s0 += lo16(q[i].x); s1 += hi16(q[i].x); s2 += lo16(q[i].z); s3 += hi16(q[i].z); }
          if (__all(ok && z.y == ep && z.w == ep && dn == ep)) break;
        }
        FENCE();
        float a0 = h32(s0, s2), a1 = h32(s1, s3);
        a0 = h16(a0, a0); a1 = h16(a1, a1);
        a0 += dppf<ROR4>(a0); a1 += dppf<ROR4>(a1); a0 += dppf<ROR8>(a0); a1 += dppf<ROR8>(a1);
        const u32 zz = lane < 32 ? z.x : z.z, gg = lane < 32 ? gb.x : gb.y;
        const u32 o = pk(lo16(zz) + lo16(gg) * (rbf(a0) * sg), hi16(zz) + hi16(gg) * (rbf(a1) * sg));
        if ((lane & 28) == 0) __builtin_amdgcn_raw_buffer_store_b32(o, mk(p.out, 2048 * 896), col * 2 + (lane >> 5) * 4, (2047 - (key & 0x7ff)) * 896, SC1);
        if (f == 0 && lane == 0) __builtin_amdgcn_raw_buffer_store_b32(ep, rw, 0, WS_EPOCH, SC1);
      } else {
      }
    }
    asm volatile("s_endpgm" : "=v"(junk), "=v"(pw), "=v"(vx)::"memory");
  }
  if (w < 4) {
    CAddr ca;
    const u32 r = lane & 15, g = lane >> 4, oob = 0x80000000u, f = (r >> 3) * 7168 + (r & 7) * 128 + ((g ^ (r & 7)) * 16);
    ca.l0 = L_H + w * 14336 + f; ca.l1 = ca.l0 ^ 64;
    ca.w0 = f; ca.w1 = f ^ 64; ca.w2 = f + 57344; ca.w3 = ca.w2 ^ 64; ca.cs = L_MOD + g * 32;
    ca.vo0 = (lane & 31) * 16 + (lane >> 5) * 1024; ca.vo1 = lane < 32 ? ca.vo0 : oob;
    u32 f1 = WS_F1 + ((half * 4 + ec) * 32 + sig) * 128, f2 = WS_F2 + (tt * 16 + (half * 2 + kh) * 4 + ec) * 128;
    u32 bs = WS_AB + ((half * 64 + kh * 32) * 28 + ec * 7) * 1024, ps = WS_PART + tt * 16 * 28672 + (half * 2 + kh) * 7168 + ec * 7 * 256;
    asm volatile("" : "+s"(f1), "+s"(f2), "+s"(bs), "+s"(ps));
    ca.vf1 = lane < 1 ? f1 + lane * 32768 : oob; ca.vf2 = lane < 1 ? f2 + lane * 32768 : oob;
    u32 so0 = WS_AB + ((half * 64 + sig * 2 + (w >> 1)) * 28 + ec * 7) * 1024 + (w & 1) * 512, so1 = so0 + 4096, vep = ep;
    asm volatile("" : "+v"(ca.w0), "+v"(ca.w1), "+v"(ca.w2), "+v"(ca.w3), "+v"(ca.l0), "+v"(ca.l1), "+v"(ca.cs), "+v"(ca.vo0), "+v"(ca.vo1), "+v"(ca.vf1), "+v"(ca.vf2), "+v"(vep));
    asm volatile("" : "+s"(so0), "+s"(so1));
    const u32 vf1 = ca.vf1, vf2 = ca.vf2;
    u32 fw = L_CTL + C_WL + min(lane & 3, 2) * 4;
    asm volatile("" : "+v"(fw));
    asm volatile("s_waitcnt vmcnt(0)" : "+v"(junk)::"memory");
    f32x2 n[14][4];
    ln1(ca.l0, ca.l1, n);
    small_gemm(rw, n, ca.w0, ca.w1, ca.w2, ca.w3, ca.cs, fw, ca.vo0, ca.vo1, so0, so1);
    marki<C_PROG, G_H>(pw);
    flag_last<C_N1>(rw, vf1, vep);
    mark<C_PROG>(pw, G_X1);
    tail_issue(p, b, w, lane, ti);
    asm volatile(".Lpc_mid:");
    if (w == 0) big_gemm<0>(p, L, lane, bs, ps);
    else if (w == 1) big_gemm<1>(p, L, lane, bs, ps);
    else if (w == 2) big_gemm<2>(p, L, lane, bs, ps);
    else big_gemm<3>(p, L, lane, bs, ps);
    flag_last<C_N2>(rw, vf2, vep);
    mark<C_PROG>(pw, G_X2);
  } else {
    {
      const rsrc_t rd = mk(wdh, 448 * 896);
      u32 uw = w - 4 + 2 * (j >> 2), sw = ec * 100352;
      pin2(uw, sw);
      static_for<0, 5>([&](auto I) {
        constexpr int i = I.value;
        if constexpr (i == WE) { if (prod) spin_words(L_CTL + C_S); }
        if constexpr (i == PGS) marki<C_XL - 16, 1>(pw);
        if (i < 4 || w < 6) {
          const u32 t = uw + 3 * i, t1 = t >= 14 ? t - 14 : t;
          u32 db = (t1 >= 14 ? t1 - 14 : t1) * 7168, so = db + sw;
          pin2(so, db);
          group(rd, so, db);
        }
      });
      if (PGS == 5) marki<C_XL - 16, 1>(pw);
    }
    asm volatile("s_waitcnt vmcnt(0)" : "+v"(junk)::"memory");
    marki<C_WL - 16, 1>(pw);
    if (w == 4) {
      const rsrc_t ra = mk(half ? p.sim : p.sre, 2048 * 4096);
      const u32 r7 = lane >> 3;
      u32 sa = tt * 128 * 4096 + kh * 2048 + slice_of(0) * 128;
      asm volatile("" : "+s"(sa));
      u32 va[16];
#pragma unroll
      for (int pi = 0; pi < 16; ++pi) {
        va[pi] = (8 * (r7 >> 2) + (r7 & 3) + 32 * (pi >> 2) + 16 * (pi & 1) + 4 * ((pi >> 1) & 1)) * 4096 + (((lane & 7) ^ r7) * 16);
        asm volatile("" : "+v"(va[pi]));
      }
      auto slices = [&](auto K0, auto K1) {
        static_for<K0.value * 16, K1.value * 16>([&](auto I) {
          constexpr int k = I.value >> 4, pi = I.value & 15;
          dmac<(k % 9) * 16384 + pi * 1024, 0>(va[pi], ra, sa);
          if constexpr (pi == 15 && k < 15) asm volatile("s_add_u32 %0, %0, %1" : "+s"(sa) : "i"((slice_of(k + 1) - slice_of(k)) * 128) : "scc");
        });
      };
      slices(IC<0>{}, IC<NPRE>{});
      wait_prog(L, G_H, lane);
      static_for<NPRE, 9>([&](auto K) {
        slices(K, IC<K.value + 1>{});
        if constexpr (K.value >= LAG) { wait_v<16 * LAG>(); mark<C_FULL>(L_CTL, K.value + 1 - LAG); }
      });
      VM0();
      mark<C_FULL>(L_CTL, 9);
      static_for<9, 16>([&](auto K) {
        constexpr int k = K.value;
        wait_prog(L, G_RING + k - 9, lane);
        slices(K, IC<k + 1>{});
        if constexpr (k > 9) { wait_v<16>(); mark<C_FULL>(L_CTL, k); }
      });
      VM0();
      mark<C_FULL>(L_CTL, 16);
    } else {
      wait_words(L, L_CTL + C_X1, TLX);
      if (TB < 8 && w == 5) {
        u32 tsink = 0;
        const rsrc_t ra = mk(half ? p.sim : p.sre, 2048 * 4096);
        const u32 va = (lane & 31) * 4096 + (lane >> 5) * 1024, sa = (tt * 128 + ec * 32) * 4096 + kh * 2048;
        static_for<TB, 8>([&](auto I) {
          if (TL) wait_prog(L, G_RING - 1 + TLO + TL * (I.value - TB), lane);
          u32 k_ = tsink; touch<slice_of(2 * I.value) * 128>(k_, va, ra, sa); if (TL) asm volatile("s_waitcnt vmcnt(0)" : "+v"(k_)::"memory"); tsink = k_;
        });
        asm volatile("s_waitcnt vmcnt(0)" : "+v"(tsink)::"memory");
      }
    }
    tail_issue(p, b, w, lane, ti);
    asm volatile("s_waitcnt vmcnt(0)" : "+v"(ti.xa), "+v"(ti.xb), "+v"(ti.wr)::"memory");
  }

  {
    const int ci = lane & 3, e0 = 64 * w + 4 * (lane >> 2);
    const v2u xa = ti.xa, xb = ti.xb, wr = ti.wr;
    const v2u ga = ldsld<v2u>(L, L_MOD + 2 * 896 + e0 * 2), sb = ldsld<v2u>(L, L_MOD + 3 * 896 + e0 * 2), cb = ldsld<v2u>(L, L_MOD + 4 * 896 + e0 * 2), gb = ldsld<v2u>(L, L_MOD + 5 * 896 + e0 * 2);
    float gaf[4] = {lo16(ga.x), hi16(ga.x), lo16(ga.y), hi16(ga.y)}, sbf[4] = {lo16(sb.x), hi16(sb.x), lo16(sb.y), hi16(sb.y)};
    float cbf[4] = {lo16(cb.x), hi16(cb.x), lo16(cb.y), hi16(cb.y)}, gbf[4] = {lo16(gb.x), hi16(gb.x), lo16(gb.y), hi16(gb.y)};
    float wrf[4] = {lo16(wr.x), hi16(wr.x), lo16(wr.y), hi16(wr.y)};
    float va[4] = {lo16(xa.x), hi16(xa.x), lo16(xa.y), hi16(xa.y)}, vb[4] = {lo16(xb.x), hi16(xb.x), lo16(xb.y), hi16(xb.y)};
    float wp[4], Wp = 0.f, Cp = 0.f;
#pragma unroll
    for (int i = 0; i < 4; ++i) { wp[i] = cbf[i] * wrf[i]; Wp += wp[i]; Cp += sbf[i] * wrf[i]; }
#pragma unroll
    for (int i = 0; i < 4; ++i) asm volatile("" : "+v"(wp[i]), "+v"(va[i]), "+v"(vb[i]), "+v"(gaf[i]));
    asm volatile("" : "+v"(Wp), "+v"(Cp));
    if (w == 6) {
      wait_prog(L, G_X2, lane);
      poll64(p, WS_F2 + (b >> 4) * 16 * 128, ep, lane, WS_PART + b * 28672);
    }
    BAR();
    v4u q[4];
#pragma unroll
    for (int s = 0; s < 4; ++s) q[s] = __builtin_amdgcn_raw_buffer_load_b128(rw, lane * 16, WS_PART + b * 28672 + s * 7168 + w * 1024, SC1);
#pragma unroll
    for (int i = 0; i < 2; ++i) {
      va[2 * i] += gaf[2 * i] * ((lo16(q[0][i]) + lo16(q[1][i])) - (lo16(q[2][i]) + lo16(q[3][i])));
      va[2 * i + 1] += gaf[2 * i + 1] * ((hi16(q[0][i]) + hi16(q[1][i])) - (hi16(q[2][i]) + hi16(q[3][i])));
      vb[2 * i] += gaf[2 * i] * ((lo16(q[0][2 + i]) + lo16(q[1][2 + i])) - (lo16(q[2][2 + i]) + lo16(q[3][2 + i])));
      vb[2 * i + 1] += gaf[2 * i + 1] * ((hi16(q[0][2 + i]) + hi16(q[1][2 + i])) - (hi16(q[2][2 + i]) + hi16(q[3][2 + i])));
    }
    float s1a = (va[0] + va[1]) + (va[2] + va[3]), s1b = (vb[0] + vb[1]) + (vb[2] + vb[3]);
    float s2a = (va[0] * va[0] + va[1] * va[1]) + (va[2] * va[2] + va[3] * va[3]), s2b = (vb[0] * vb[0] + vb[1] * vb[1]) + (vb[2] * vb[2] + vb[3] * vb[3]);
    float s3a = (va[0] * wp[0] + va[1] * wp[1]) + (va[2] * wp[2] + va[3] * wp[3]), s3b = (vb[0] * wp[0] + vb[1] * wp[1]) + (vb[2] * wp[2] + vb[3] * wp[3]);
    float v0 = h16(h32(s1a, s1b), h32(s2a, s2b)), v1 = h16(h32(s3a, s3b), h32(Wp, Cp));
    v0 += dppf<ROR4>(v0); v1 += dppf<ROR4>(v1); v0 += dppf<ROR8>(v0); v1 += dppf<ROR8>(v1);
    if ((lane & 12) == 0) {
      const u32 o = L_ST + ((lane >> 4) & 1) * 256 + w * 32 + (lane >> 5) * 16 + ci * 4;
      ldsst<float>(L, o, v0); ldsst<float>(L, o + 512, v1);
    }
    BAR();
    float t[4];
#pragma unroll
    for (int k = 0; k < 4; ++k) { t[k] = ldsld<float>(L, L_ST + k * 256 + lane * 4); t[k] = lane < 56 ? t[k] : 0.f; }
#pragma unroll
    for (int k = 0; k < 4; ++k) { t[k] += dppf<ROR8>(t[k]); t[k] = h16(t[k], t[k]); t[k] = h32(t[k], t[k]); }
    const bool up = lane & 4;
    const float oth = dppf<ROR4>(t[3]), Wt = up ? oth : t[3], Ct = up ? t[3] : oth;
    const float mu = t[0] * (1.f / 448.f);
    const float rs = __builtin_amdgcn_rsqf(fmaxf(t[1] * (1.f / 448.f) - mu * mu, 0.f) + 1e-5f);
    const float rr = rs * (t[2] - mu * Wt) + Ct;
    u32 key = __float_as_uint(rr);
    key = (key & 0x80000000u) ? ~key : (key | 0x80000000u);
    key = (key & ~0x7ffu) | (2047 - (b * 8 + (lane & 7)));
    key = umax(key, dppu<XOR1>(key)); key = umax(key, dppu<XOR2>(key)); key = umax(key, dppu<HMIR>(key));
    if (w == 0 && lane < 8) __builtin_amdgcn_raw_buffer_store_b64(v2u{key, ep}, rw, lane * 32768, WS_KEYR + (b / KPL) * 128 + (b % KPL) * 8, SC1);
    const float mo = dppf<ROR4>(mu), ro = dppf<ROR4>(rs);
    const float mua = up ? mo : mu, mub = up ? mu : mo, rsa = up ? ro : rs, rsb = up ? rs : ro;
    float ha[4], hb[4];
#pragma unroll
    for (int i = 0; i < 4; ++i) { ha[i] = (va[i] - mua) * rsa * cbf[i] + sbf[i]; hb[i] = (vb[i] - mub) * rsb * cbf[i] + sbf[i]; }
    const u32 best = (2047 - (__builtin_amdgcn_readfirstlane(key) & 0x7ff)) & 7;
    const u32 vc = ci == (best & 3) ? e0 * 4 : 0x80000000u, cs = WS_CAND + b * 3584;
    const bool lo4 = best < 4;
    bstp<0>(v4u{lo4 ? pk(ha[0], ha[1]) : pk(hb[0], hb[1]), ep, lo4 ? pk(ha[2], ha[3]) : pk(hb[2], hb[3]), ep}, vc, rw, cs);
    const u32 za0 = pk(va[0] + gbf[0] * ha[0], va[1] + gbf[1] * ha[1]), za1 = pk(va[2] + gbf[2] * ha[2], va[3] + gbf[3] * ha[3]);
    const u32 zb0 = pk(vb[0] + gbf[0] * hb[0], vb[1] + gbf[1] * hb[1]), zb1 = pk(vb[2] + gbf[2] * hb[2], vb[3] + gbf[3] * hb[3]);
    bstp<1792>(v4u{lo4 ? za0 : zb0, ep, lo4 ? za1 : zb1, ep}, vc, rw, cs);
    const v4u zo = {(u32)__builtin_amdgcn_update_dpp(za0, zb0, SHR4, 0xf, 0xa, false), (u32)__builtin_amdgcn_update_dpp(za1, zb1, SHR4, 0xf, 0xa, false),
                    (u32)__builtin_amdgcn_update_dpp(zb0, za0, SHL4, 0xf, 0x5, false), (u32)__builtin_amdgcn_update_dpp(zb1, za1, SHL4, 0xf, 0x5, false)};
    bstp<0>(zo, (lane & 7) * 896 + 128 * w + (lane >> 3) * 16, mk(p.out, 2048 * 896), b * 7168);
    marki<C_H, 2>(pw);
    flag_after_drain(rw, lane == 0 ? WS_DONE + b * 128 + w * 4 : 0x80000000u, ep);
    asm volatile(".Lpc_last:");
  }
}

extern "C" void kernel_launch(void* const* d_in, const int* in_sizes, int n_in, void* d_out, int out_size, void* d_ws, size_t ws_size, hipStream_t stream) {
  Params p;
  p.x = (const u16*)d_in[0]; p.sre = (const u16*)d_in[1]; p.sim = (const u16*)d_in[2]; p.dre = (const u16*)d_in[3]; p.dim = (const u16*)d_in[4];
  p.wr = (const u16*)d_in[6]; p.wg = (const u16*)d_in[7]; p.wu = (const u16*)d_in[8]; p.wd = (const u16*)d_in[9]; p.c = (const u16*)d_in[10]; p.wmod = (const u16*)d_in[11];
  p.out = (u16*)d_out; p.ws = (unsigned char*)d_ws;
  (void)hipFuncSetAttribute((const void*)k_fused, hipFuncAttributeMaxDynamicSharedMemorySize, L_TOTAL);
  k_fused<<<256, 512, L_TOTAL, stream>>>(p);
}
```
